```python
import torch

def _softmax(x, axis=-1):
    return torch.softmax(x, dim=axis)
def _gelu(x):
    return torch.nn.functional.gelu(x, approximate="tanh")
def _silu(x):
    return torch.nn.functional.silu(x)
def _sigmoid(x):
    return torch.sigmoid(x)
def _layernorm(x, axis=-1, eps=1e-5):
    m = x.mean(dim=axis, keepdim=True)
    v = x.var(dim=axis, keepdim=True, unbiased=False)
    return (x - m) / torch.sqrt(v + eps)
def _scatter_add(tgt, idx, src, axis=0):
    return tgt.clone().index_add_(axis, idx, src)
def _segment_sum(src, seg, n):
    out = torch.zeros((n,) + src.shape[1:], dtype=src.dtype)
    return out.index_add_(0, seg, src)
def _pad(x, pw):
    flat = tuple(v for pair in reversed(pw) for v in pair)
    return torch.nn.functional.pad(x, flat)

kernel_name = 'gated_mix__mha_block__mixer_s_mlp__prompt'

def setup_inputs():

    _w = (lambda a: (lambda t: t.to(torch.bfloat16) if t.is_floating_point() else t)(torch.as_tensor(a)))
    def _A_setup():
        B, S, D, H = 1, 2048, 1024, 32
        return {
            "x": _w((lambda *s: torch.randn(*s, dtype=torch.bfloat16) * 0.1)(B, S, D)),
            "wqkv": _w((lambda *s: torch.randn(*s, dtype=torch.bfloat16) * 0.4)(D, 3 * D)),
            "wo": _w((lambda *s: torch.randn(*s, dtype=torch.bfloat16) * 0.4)(D, D)),
        }
    _A_in = _A_setup()

    def _B_setup():
        B, S, D, F = 1, 2048, 1024, 3584
        return {
            "x":  _w((lambda *s: torch.randn(*s, dtype=torch.bfloat16) * 0.1)(B, S, D)),
            "w1": _w((lambda *s: torch.randn(*s, dtype=torch.bfloat16) * 3.2)(D, D) * (D ** -0.5)),
            "w2": _w((lambda *s: torch.randn(*s, dtype=torch.bfloat16) * 3.2)(D, D) * (D ** -0.5)),
            "w3": _w((lambda *s: torch.randn(*s, dtype=torch.bfloat16) * 3.2)(D, F) * (D ** -0.5)),
            "w4": _w((lambda *s: torch.randn(*s, dtype=torch.bfloat16) * 3.2)(F, D) * (F ** -0.5)),
        }
    _B_in = _B_setup()

    return {
        "A_x": _A_in["x"],
        "A_wqkv": _A_in["wqkv"],
        "A_wo": _A_in["wo"],
        "B_x": _B_in["x"],
        "B_w1": _B_in["w1"],
        "B_w2": _B_in["w2"],
        "B_w3": _B_in["w3"],
        "B_w4": _B_in["w4"],
        "gate_w": _w((lambda *s: torch.randn(*s, dtype=torch.bfloat16) * 0.4)(1024) * 16.0),
    }

def reference(A_x, A_wqkv, A_wo, B_x, B_w1, B_w2, B_w3, B_w4, gate_w):
    def _A_ref(x, wqkv, wo):
        B, S, D = x.shape
        H = 32
        Dh = D // H
        qkv = (lambda x, *d: x.permute(*d))((x @ wqkv).reshape(B, S, 3, H, Dh), 2, 0, 3, 1, 4)
        q, k, v = qkv[0], qkv[1], qkv[2]
        scores = torch.einsum("bhqd,bhkd->bhqk", q, k) * (Dh ** -0.5)
        p = _softmax(scores, axis=-1)
        o = torch.einsum("bhqk,bhkd->bhqd", p, v)
        return (lambda x, *d: x.permute(*d))(o, 0, 2, 1, 3).reshape(B, S, D) @ wo

    def _B_ref(x, w1, w2, w3, w4):
        B, S, D = x.shape
        h = _gelu(_layernorm(x, axis=-1) @ w1)
        c = D // 4
        g0 = _pad(h[:, :, 0*c:1*c], ((0, 0), (1, 0), (0, 0)))[:, :S, :]
        g1 = _pad(h[:, :, 1*c:2*c], ((0, 0), (0, 1), (0, 0)))[:, 1:, :]
        g2 = _pad(h[:, :, 2*c:3*c], ((0, 0), (2, 0), (0, 0)))[:, :S, :]
        g3 = _pad(h[:, :, 3*c:   ], ((0, 0), (0, 2), (0, 0)))[:, 2:, :]
        hs = torch.concatenate([g0, g1, g2, g3], axis=-1) @ w2
        x1 = x + hs
        h2 = _gelu(_layernorm(x1, axis=-1) @ w3) @ w4
        return x1 + h2

    x = A_x
    g = _sigmoid((x * gate_w).sum(-1, keepdims=True))
    a = _A_ref(x, A_wqkv, A_wo)
    b = _B_ref(x, B_w1, B_w2, B_w3, B_w4)
    return g * a + (1 - g) * b
```

```cpp
#include <hip/hip_runtime.h>
#include <hip/hip_bf16.h>
#include <stdint.h>

typedef unsigned short u16;
typedef short bf16x8 __attribute__((ext_vector_type(8)));
typedef short s16x4 __attribute__((ext_vector_type(4)));
typedef float f32x16 __attribute__((ext_vector_type(16)));
typedef float f32x4 __attribute__((ext_vector_type(4)));
typedef float f32x2 __attribute__((ext_vector_type(2)));
typedef unsigned u32x4 __attribute__((ext_vector_type(4)));
typedef __attribute__((address_space(3))) char* lds_ptr;
typedef __attribute__((address_space(1))) unsigned gu32;
#define DEV __device__ __forceinline__
#define LDS_AS __attribute__((address_space(3)))
#define SBAR() __builtin_amdgcn_sched_barrier(0)
#define PIN(x) asm volatile("" : "+v"(x))
#define MFMA(a, b, c) __builtin_amdgcn_mfma_f32_32x32x16_bf16(a, b, c, 0, 0, 0)
#define MFMA16(a, b, c) __builtin_amdgcn_mfma_f32_16x16x32_bf16(a, b, c, 0, 0, 0)
#define BAR() asm volatile("s_barrier" ::: "memory")
#define WAIT_BAR(N) asm volatile("s_waitcnt vmcnt(" #N ") lgkmcnt(0)\n\ts_barrier" ::: "memory")
#define LG_BAR() asm volatile("s_waitcnt lgkmcnt(0)\n\ts_barrier" ::: "memory")

constexpr int S_ = 2048, D_ = 1024, F_ = 3584;
constexpr int KB4 = (F_ + D_) / 16;
constexpr size_t MB = 1u << 20;
constexpr size_t WS_FLAG = 0;
constexpr size_t WS_G = 4096;
constexpr size_t WS_CS3 = 32768;
constexpr size_t WS_ST = 65536;
constexpr size_t WS_Q = 1 * MB;
constexpr size_t WS_KV = 5 * MB;
constexpr size_t WS_HS = 13 * MB;
constexpr size_t WS_X1 = 17 * MB;
constexpr size_t WS_A4 = 21 * MB;
constexpr size_t WS_EX = 40 * MB;

DEV unsigned cvtpk(float lo, float hi) {
    unsigned r;
    asm("v_cvt_pk_bf16_f32 %0, %1, %2" : "=v"(r) : "v"(lo), "v"(hi));
    return r;
}
DEV float bflo(unsigned u) { return __uint_as_float(u << 16); }
DEV float bfhi(unsigned u) { return __uint_as_float(u & 0xffff0000u); }
DEV float rbf(float f) { return bflo(cvtpk(f, f) & 0xffffu); }
DEV u32x4 pack8(const f32x16& a, int s, float sc) {
    u32x4 r;
    r[0] = cvtpk(a[8 * s + 0] * sc, a[8 * s + 1] * sc);
    r[1] = cvtpk(a[8 * s + 2] * sc, a[8 * s + 3] * sc);
    r[2] = cvtpk(a[8 * s + 4] * sc, a[8 * s + 5] * sc);
    r[3] = cvtpk(a[8 * s + 6] * sc, a[8 * s + 7] * sc);
    return r;
}
DEV u32x4 pack8n(const f32x16& a, int s) {
    u32x4 r;
    r[0] = cvtpk(a[8 * s + 0], a[8 * s + 1]);
    r[1] = cvtpk(a[8 * s + 2], a[8 * s + 3]);
    r[2] = cvtpk(a[8 * s + 4], a[8 * s + 5]);
    r[3] = cvtpk(a[8 * s + 6], a[8 * s + 7]);
    return r;
}
DEV float xhalf_sum(float v) {
    auto rr = __builtin_amdgcn_permlane32_swap(__float_as_uint(v), __float_as_uint(v), false, false);
    return __uint_as_float(rr[0]) + __uint_as_float(rr[1]);
}
DEV float xhalf_max(float v) {
    auto rr = __builtin_amdgcn_permlane32_swap(__float_as_uint(v), __float_as_uint(v), false, false);
    return __builtin_fmaxf(__uint_as_float(rr[0]), __uint_as_float(rr[1]));
}
DEV float gelu_t(float u) {
    const float c0 = -2.0f * 0.7978845608028654f * 1.4426950408889634f;
    const float c1 = c0 * 0.044715f;
    float y = u * __builtin_fmaf(u * u, c1, c0);
    float e = __builtin_amdgcn_exp2f(y);
    return u * __builtin_amdgcn_rcpf(1.0f + e);
}
DEV bf16x8 ld128(lds_ptr p) { return *(const LDS_AS bf16x8*)p; }
typedef short v4i16_t __attribute__((ext_vector_type(4)));
DEV bf16x8 ldtr(lds_ptr p, int toff) {
    v4i16_t a = __builtin_amdgcn_ds_read_tr16_b64_v4i16((LDS_AS v4i16_t*)p);
    v4i16_t b = __builtin_amdgcn_ds_read_tr16_b64_v4i16((LDS_AS v4i16_t*)(p + toff));
    return (bf16x8){a[0], a[1], a[2], a[3], b[0], b[1], b[2], b[3]};
}
DEV void glds_x4w2(unsigned x0, unsigned x1, unsigned x2, unsigned x3, const void* xs, unsigned w0, const void* ws0,
                   unsigned w1, const void* ws1, unsigned dx, unsigned dw0, unsigned dw1) {
    unsigned sv;
    asm volatile(
        "s_mov_b32 %0, m0\n\ts_mov_b32 m0, %10\n\ts_nop 0\n\t"
        "global_load_lds_dwordx4 %1, %5\n\t"
        "global_load_lds_dwordx4 %2, %5 offset:1024\n\t"
        "global_load_lds_dwordx4 %3, %5 offset:2048\n\t"
        "global_load_lds_dwordx4 %4, %5 offset:3072\n\t"
        "s_mov_b32 m0, %11\n\ts_nop 0\n\t"
        "global_load_lds_dwordx4 %6, %7\n\t"
        "s_mov_b32 m0, %12\n\ts_nop 0\n\t"
        "global_load_lds_dwordx4 %8, %9\n\t"
        "s_mov_b32 m0, %0"
        : "=&s"(sv)
        : "v"(x0), "v"(x1), "v"(x2), "v"(x3), "s"(xs), "v"(w0), "s"(ws0), "v"(w1), "s"(ws1), "s"(dx), "s"(dw0), "s"(dw1)
        : "memory");
}
DEV void glds_x4w4(unsigned x0, const void* xs, unsigned wa, unsigned wb, const void* ws0, const void* ws1, unsigned dx,
                   unsigned dw0, unsigned dw1) {
    unsigned sv;
    asm volatile(
        "s_mov_b32 %0, m0\n\ts_mov_b32 m0, %7\n\ts_nop 0\n\t"
        "global_load_lds_dwordx4 %1, %2\n\t"
        "global_load_lds_dwordx4 %1, %2 offset:1024\n\t"
        "global_load_lds_dwordx4 %1, %2 offset:2048\n\t"
        "global_load_lds_dwordx4 %1, %2 offset:3072\n\t"
        "s_mov_b32 m0, %8\n\ts_nop 0\n\t"
        "global_load_lds_dwordx4 %3, %5\n\t"
        "global_load_lds_dwordx4 %4, %5 offset:1024\n\t"
        "s_mov_b32 m0, %9\n\ts_nop 0\n\t"
        "global_load_lds_dwordx4 %3, %6\n\t"
        "global_load_lds_dwordx4 %4, %6 offset:1024\n\t"
        "s_mov_b32 m0, %0"
        : "=&s"(sv)
        : "v"(x0), "s"(xs), "v"(wa), "v"(wb), "s"(ws0), "s"(ws1), "s"(dx), "s"(dw0), "s"(dw1)
        : "memory");
}
DEV int rfl(int v) { return __builtin_amdgcn_readfirstlane(v); }

constexpr int STG_A = 49152;
template <int KIND>
DEV void kloopA(char* smem, const char* xs, unsigned xld  , const char* ws0,
                unsigned ld0, const char* ws1, unsigned ld1, int nkt, f32x16 (&acc)[2][2], f32x2& rs1, f32x2& rs2,
                f32x2& cs) {
    const int tid = threadIdx.x, lane = tid & 63, wid = rfl(tid >> 6), wm = wid & 3, wn = wid >> 2;
    const int h = lane >> 5, r32 = lane & 31;
    const unsigned lds0 = (unsigned)(uintptr_t)(lds_ptr)smem;
    unsigned xv[4];
    if (KIND == 2) {
#pragma unroll
        for (int i = 0; i < 4; ++i) xv[i] = wid * xld + lane * 16;
    } else {
#pragma unroll
        for (int i = 0; i < 4; ++i) {
            const int row = 32 * wid + 8 * i + (lane >> 3);
            xv[i] = row * xld + (((lane & 7) ^ ((row >> 1) & 7)) << 4) - i * 1024;
        }
    }
    const int wrow = 8 * wid + (lane >> 3);
    const unsigned wch = ((lane & 7) ^ (((wrow >> 1) & 1) << 2)) << 4;
    const unsigned wv0 = wrow * ld0 + wch, wv1 = wrow * ld1 + wch;
    const unsigned xstep = KIND == 2 ? 4096u : 128u, wstep0 = 64 * ld0, wstep1 = 64 * ld1;
    const unsigned dx = lds0 + wid * 4096, dw = lds0 + 32768 + wid * 1024;
#define ISSUE_A(so)                                                                                     \
    do {                                                                                                \
        glds_x4w2(xv[0], xv[1], xv[2], xv[3], xs, wv0, ws0, wv1, ws1, dx + (so), dw + (so), dw + 8192 + (so)); \
        xs += xstep; ws0 += wstep0; ws1 += wstep1;                                                      \
    } while (0)
    const int sx = KIND == 1 ? wm : 0, mx = KIND == 1 ? wn : 0;
    unsigned xb[4], wb[4];
    const int q = (lane & 15) >> 2, p = lane & 3, g1 = (lane >> 4) & 1;
    const unsigned wcol = (((4 * wn + 2 * g1 + (p >> 1)) ^ ((q >> 1) << 2)) << 4) + (p & 1) * 8;
#pragma unroll
    for (int s = 0; s < 4; ++s) {
        const int sa = s ^ sx;
        if (KIND == 2) {
            xb[s] = (2 * wm * 4 + sa) * 1024 + lane * 16;
            wb[s] = 32768 + (16 * sa + 4 * h + q) * 128 + wcol;
        } else {
            xb[s] = (64 * wm + 32 * mx + r32) * 128 + (((2 * sa + h) ^ ((r32 >> 1) & 7)) << 4);
            wb[s] = 32768 + (16 * sa + 8 * h + q) * 128 + wcol;
        }
    }
    const int mstep = KIND == 1 ? 4096 - 8192 * wn : 4096;
    constexpr int TOFF = KIND == 2 ? 1024 : 512;
    ISSUE_A(0);
    ISSUE_A(STG_A);
    WAIT_BAR(6);
    if (wn) BAR();
    int so = 0, so2 = 2 * STG_A;
    for (int t = 0; t < nkt; ++t) {
        bf16x8 xf[2][4], wf[2][4];
        lds_ptr sp = (lds_ptr)smem + so;
#pragma unroll
        for (int s = 0; s < 4; ++s) {
            wf[0][s] = ldtr(sp + wb[s], TOFF);
            xf[0][s] = ld128(sp + xb[s]);
            wf[1][s] = ldtr(sp + wb[s] + 8192, TOFF);
            xf[1][s] = ld128(sp + xb[s] + mstep);
        }
        SBAR();
        if (t + 2 < nkt) {
            ISSUE_A(so2);
            WAIT_BAR(6);
        } else {
            WAIT_BAR(0);
        }
        SBAR();
        __builtin_amdgcn_s_setprio(1);
#pragma unroll
        for (int s = 0; s < 4; ++s) {
#pragma unroll
            for (int i = 0; i < 4; ++i) {
                const int nb = i & 1, mb = i >> 1;
                if (KIND == 1 && nb == 1) acc[nb][mb] = MFMA(xf[mb][s], wf[nb][s], acc[nb][mb]);
                else acc[nb][mb] = MFMA(wf[nb][s], xf[mb][s], acc[nb][mb]);
                if (KIND == 1) {
                    const unsigned u = __builtin_bit_cast(u32x4, xf[0][s])[i];
                    const f32x2 v = {bflo(u), bfhi(u)};
                    rs1 += v;
                    rs2 = __builtin_elementwise_fma(v, v, rs2);
                    if (i == 0) {
                        const unsigned w = __builtin_bit_cast(u32x4, wf[0][0])[s];
                        cs += (f32x2){bflo(w), bfhi(w)};
                        PIN(cs);
                    }
                    PIN(rs1);
                    PIN(rs2);
                    SBAR();
                }
            }
        }
        __builtin_amdgcn_s_setprio(0);
        SBAR();
        if (!(wn && t == nkt - 1)) BAR();
        so = so == 2 * STG_A ? 0 : so + STG_A;
        so2 = so2 == 2 * STG_A ? 0 : so2 + STG_A;
    }
#undef ISSUE_A
}

constexpr int STG_B = 32768;
template <class NextW>
DEV void kloopB(char* smem, const char* xs, unsigned xld, unsigned ldw, NextW wptr, int nkt, f32x16 (&res)[2]) {
    const int tid = threadIdx.x, lane = tid & 63, wid = rfl(tid >> 6), j = wid & 3, grp = wid >> 2, wm = j & 1, wn = j >> 1;
    const int h = lane >> 5;
    const unsigned lds0 = (unsigned)(uintptr_t)(lds_ptr)smem;
    const unsigned xv = j * xld + lane * 16;
    const int wrow = 16 * j + (lane >> 3);
    const unsigned wch = ((lane & 7) ^ (((wrow >> 1) & 1) << 2)) << 4;
    const unsigned wva = wrow * ldw + wch, wvb = wva + 8 * ldw - 1024;
    const unsigned dx = lds0 + j * 4096, dw = lds0 + 16384 + j * 2048;
#define ISSUE_B(tile)                                                                                   \
    do {                                                                                                \
        const int tl_ = (tile);                                                                         \
        const char* w_ = wptr(tl_);                                                                     \
        const unsigned so_ = (tl_ & 3) * STG_B;                                                         \
        glds_x4w4(xv, xs + (size_t)tl_ * 4096, wva, wvb, w_, w_ + 128, dx + so_, dw + so_, dw + 8192 + so_); \
    } while (0)
    const int q = (lane & 15) >> 2, p = lane & 3, g1 = (lane >> 4) & 1;
    const unsigned wcol = (((4 * wn + 2 * g1 + (p >> 1)) ^ ((q >> 1) << 2)) << 4) + (p & 1) * 8;
    const unsigned xb = (2 * wm * 4) * 1024 + lane * 16, wb = 16384 + (4 * h + q) * 128 + wcol;
    f32x16 acc[2][2] = {};
    if (grp) {
        ISSUE_B(0);
        ISSUE_B(2);
        WAIT_BAR(8);
        BAR();
    } else {
        ISSUE_B(1);
        BAR();
    }
    for (int t = grp; t < nkt; t += 2) {
        bf16x8 xf[2][4], wf[2][4];
        lds_ptr sp = (lds_ptr)smem + (t & 3) * STG_B;
#pragma unroll
        for (int s = 0; s < 4; ++s) {
            wf[0][s] = ldtr(sp + wb + s * 2048, 1024);
            xf[0][s] = ld128(sp + xb + s * 1024);
            wf[1][s] = ldtr(sp + wb + s * 2048 + 8192, 1024);
            xf[1][s] = ld128(sp + xb + s * 1024 + 4096);
        }
        SBAR();
        if (t + 3 < nkt) {
            ISSUE_B(t + 3);
            WAIT_BAR(8);
        } else {
            WAIT_BAR(0);
        }
        SBAR();
        __builtin_amdgcn_s_setprio(1);
#pragma unroll
        for (int s = 0; s < 4; ++s)
#pragma unroll
            for (int i = 0; i < 4; ++i) acc[i & 1][i >> 1] = MFMA(wf[i & 1][s], xf[i >> 1][s], acc[i & 1][i >> 1]);
        __builtin_amdgcn_s_setprio(0);
        SBAR();
        if (t != nkt - 1) BAR();
    }
#undef ISSUE_B
    LDS_AS f32x4* ex = (LDS_AS f32x4*)((lds_ptr)smem + wid * 8192) + lane;
    const LDS_AS f32x4* ey = (const LDS_AS f32x4*)((lds_ptr)smem + (wid ^ 4) * 8192) + lane;
#pragma unroll
    for (int mb = 0; mb < 2; ++mb)
#pragma unroll
        for (int i = 0; i < 4; ++i) {
            const f32x16& a = grp ? acc[0][mb] : acc[1][mb];
            ex[(mb * 4 + i) * 64] = (f32x4){a[4 * i], a[4 * i + 1], a[4 * i + 2], a[4 * i + 3]};
        }
    LG_BAR();
#pragma unroll
    for (int mb = 0; mb < 2; ++mb)
#pragma unroll
        for (int i = 0; i < 4; ++i) {
            const f32x4 o = ey[(mb * 4 + i) * 64];
            const f32x16& a = grp ? acc[1][mb] : acc[0][mb];
#pragma unroll
            for (int e = 0; e < 4; ++e) res[mb][4 * i + e] = a[4 * i + e] + o[e];
        }
}

__global__ __launch_bounds__(512, 2) void k_gemm1(const u16* __restrict__ x, const u16* __restrict__ wqkv,
                                                  const u16* __restrict__ w1, const u16* __restrict__ gate_w,
                                                  char* __restrict__ ws) {
    extern __shared__ __attribute__((aligned(16))) char smem[];
    const int b = blockIdx.x, xcd = b & 7, jb = b >> 3, mt = 4 * (xcd & 1) + (jb & 3), nt = 8 * (xcd >> 1) + (jb >> 2);
    const int jj = nt >> 1;
    const int tid = threadIdx.x, lane = tid & 63, wid = rfl(tid >> 6), wm = wid & 3, wn = wid >> 2;
    const int h = lane >> 5, r32 = lane & 31;
    f32x16 acc[2][2] = {};
    f32x2 rs1 = {0.f, 0.f}, rs2 = {0.f, 0.f}, cs = {0.f, 0.f};
    const char* xs = (const char*)(x + (size_t)mt * 256 * D_);
    if (b == 0 && tid < 256) ((unsigned*)(ws + WS_FLAG))[tid] = 0u;
    if (nt & 1) {
        const int grow = 16 * (mt * 16 + jj) + 2 * wid;
        u32x4 gx[2][2], gw[2];
#pragma unroll
        for (int i = 0; i < 2; ++i) {
            gw[i] = *(const u32x4*)(gate_w + i * 512 + lane * 8);
            gx[0][i] = *(const u32x4*)(x + (size_t)grow * D_ + i * 512 + lane * 8);
            gx[1][i] = *(const u32x4*)(x + (size_t)(grow + 1) * D_ + i * 512 + lane * 8);
        }
        kloopA<0>(smem, xs, 2048, (const char*)(wqkv + 64 * jj), 6144, (const char*)(wqkv + 1024 + 64 * jj), 6144, 16, acc,
                  rs1, rs2, cs);
        const float qsc = 0.17677669529663687f * 1.4426950408889634f;
        const int head = 2 * jj + wn;
#pragma unroll
        for (int mb = 0; mb < 2; ++mb) {
            const int mblk = 8 * mt + 2 * wm + mb;
#pragma unroll
            for (int s = 0; s < 2; ++s) {
                *(u32x4*)(ws + WS_Q + ((size_t)(mblk * 64 + 2 * head + s) * 64 + lane) * 16) = pack8(acc[0][mb], s, qsc);
                *(u32x4*)(ws + WS_KV + ((size_t)((head * 64 + mblk) * 4 + s) * 64 + lane) * 16) = pack8n(acc[1][mb], s);
            }
        }
#pragma unroll
        for (int r = 0; r < 2; ++r) {
            float z = 0.f;
#pragma unroll
            for (int i = 0; i < 2; ++i)
#pragma unroll
                for (int e = 0; e < 4; ++e) {
                    const unsigned pr = cvtpk(bflo(gx[r][i][e]) * bflo(gw[i][e]), bfhi(gx[r][i][e]) * bfhi(gw[i][e]));
                    z += bflo(pr) + bfhi(pr);
                }
#pragma unroll
            for (int o = 32; o > 0; o >>= 1) z += __shfl_xor(z, o);
            z = rbf(z);
            const float g = rbf(1.0f / (1.0f + __expf(-z)));
            if (lane == 0) *(f32x2*)(ws + WS_G + (size_t)(grow + r) * 8) = (f32x2){g, rbf(1.0f - g)};
        }
    } else {
        kloopA<1>(smem, xs, 2048, (const char*)(w1 + 64 * jj), 2048, (const char*)(wqkv + 2048 + 64 * jj), 6144, 16, acc,
                  rs1, rs2, cs);
        LDS_AS f32x2* st = (LDS_AS f32x2*)(lds_ptr)smem;
        LDS_AS float* csl = (LDS_AS float*)((lds_ptr)smem + 2048);
        {
            const float s1 = xhalf_sum(rs1[0] + rs1[1]), s2 = xhalf_sum(rs2[0] + rs2[1]), c = xhalf_sum(cs[0] + cs[1]);
            const float mean = s1 * (1.0f / D_);
            const float var = __builtin_fmaxf(s2 * (1.0f / D_) - mean * mean, 0.f);
            if (h == 0) {
                st[64 * wm + 32 * wn + r32] = (f32x2){mean, __builtin_amdgcn_rsqf(var + 1e-5f)};
                csl[wm * 64 + 32 * wn + r32] = c;
            }
        }
        const int head = 2 * jj + wn;
#define V_EPI()                                                                                                     \
    _Pragma("unroll") for (int mb = 0; mb < 2; ++mb) {                                                              \
        const int mblk = 8 * mt + 2 * wm + (mb ^ wn);                                                               \
        _Pragma("unroll") for (int s = 0; s < 2; ++s)                                                               \
            *(u32x4*)(ws + WS_KV + ((size_t)((head * 64 + mblk) * 4 + 2 + s) * 64 + lane) * 16) = pack8n(acc[1][mb], s); \
    }
        if (!wn) { V_EPI(); }
        LG_BAR();
        float c16[16];
#pragma unroll
        for (int i = 0; i < 4; ++i) {
            f32x4 c = *(const LDS_AS f32x4*)(csl + 32 * wn + 8 * i + 4 * h);
#pragma unroll
            for (int w = 1; w < 4; ++w) c += *(const LDS_AS f32x4*)(csl + w * 64 + 32 * wn + 8 * i + 4 * h);
#pragma unroll
            for (int e = 0; e < 4; ++e) c16[4 * i + e] = c[e];
        }
        const int quarter = jj >> 2;
        const int sh = quarter == 0 ? 1 : quarter == 1 ? -1 : quarter == 2 ? 2 : -2;
#pragma unroll
        for (int mb = 0; mb < 2; ++mb) {
            const int lrow = 64 * wm + 32 * (mb ^ wn) + r32;
            const f32x2 ms = st[lrow];
            const int t = mt * 256 + lrow + sh, tw = t & (S_ - 1);
            const float keep = t == tw ? 1.0f : 0.0f;
            f32x16 hv;
#pragma unroll
            for (int r = 0; r < 16; ++r) hv[r] = gelu_t(__builtin_fmaf(-ms[0], c16[r], acc[0][mb][r]) * ms[1]);
#pragma unroll
            for (int s = 0; s < 2; ++s) {
                u32x4 v = pack8n(hv, s);
                if (keep == 0.0f) v = (u32x4){0u, 0u, 0u, 0u};
                *(u32x4*)(ws + WS_HS + ((size_t)((tw >> 5) * 64 + 4 * jj + 2 * wn + s) * 64 + h * 32 + (tw & 31)) * 16) = v;
            }
        }
        if (wn) { V_EPI(); }
#undef V_EPI
    }
}

template <int NW>
__global__ __launch_bounds__(NW * 64, 2) void k_attn(char* __restrict__ ws) {
    extern __shared__ __attribute__((aligned(16))) char smem[];
    constexpr int KS = NW / 2, NT = 64 / KS;
    const int b = blockIdx.x, xcd = b & 7, jb = b >> 3, head = 4 * xcd + (jb >> 3), qt = jb & 7;
    const int tid = threadIdx.x, lane = tid & 63, wid = rfl(tid >> 6), qg = wid & 1, ks = wid >> 1;
    const int mblk0 = qt * 8 + qg * 4;
    const bf16x8* qp = (const bf16x8*)(ws + WS_Q + (size_t)(mblk0 * 64 + 2 * head) * 1024) + lane;
    const bf16x8* kv = (const bf16x8*)(ws + WS_KV + (size_t)(head * 64 + ks * NT) * 4096) + lane;
    bf16x8 Q[4][2], Ka[2], Kb[2], Va[2], Vb[2];
    Ka[0] = kv[0]; Ka[1] = kv[64];
#pragma unroll
    for (int i = 0; i < 4; ++i) { Q[i][0] = qp[i * 64 * 64]; Q[i][1] = qp[i * 64 * 64 + 64]; }
    Va[0] = kv[128]; Va[1] = kv[192];
    Kb[0] = kv[256]; Kb[1] = kv[256 + 64];
    Vb[0] = kv[256 + 128]; Vb[1] = kv[256 + 192];
    bf16x8 sel[4];
#pragma unroll
    for (int i = 0; i < 4; ++i) {
        const short v = ((lane & 3) == i && ((lane >> 2) & 1) == ((lane >> 4) & 1)) ? (short)0x3F80 : (short)0;
        sel[i] = (bf16x8){v, v, v, v, v, v, v, v};
        PIN(sel[i]);
    }
    float mref = -INFINITY;
#pragma unroll
    for (int i = 0; i < 4; ++i) {
        f32x16 s = MFMA(Ka[0], Q[i][0], (f32x16){});
        s = MFMA(Ka[1], Q[i][1], s);
#pragma unroll
        for (int r = 0; r < 16; r += 2) mref = __builtin_fmaxf(__builtin_fmaxf(s[r], s[r + 1]), mref);
    }
    mref = xhalf_max(mref);
    f32x16 C;
#pragma unroll
    for (int r = 0; r < 16; ++r) C[r] = -mref;
    PIN(C);
    f32x16 O[4] = {}, SA, SB;
    f32x4 L = {};
    u32x4 PA[2] = {}, PB[2] = {};
    SA = MFMA(Ka[0], Q[0][0], C);
    SA = MFMA(Ka[1], Q[0][1], SA);
#define EX(X, i) X[i] = __builtin_amdgcn_exp2f(X[i])
#define CV(P, X, k) P[(k) >> 2][(k) & 3] = cvtpk(X[2 * (k)], X[2 * (k) + 1])
#define BC(x) __builtin_bit_cast(bf16x8, x)
#define STEP(Sc, Sn, Pc, Pp, Kn, qn, Vp, bp)                                                                    \
    do {                                                                                                        \
        SBAR();                                                                                                 \
        Sn = MFMA(Kn[0], Q[qn][0], C); EX(Sc, 0); EX(Sc, 1); EX(Sc, 2); PIN(Sc); SBAR();                        \
        O[bp] = MFMA(Vp[0], BC(Pp[0]), O[bp]); EX(Sc, 3); EX(Sc, 4); EX(Sc, 5); PIN(Sc); SBAR();                \
        Sn = MFMA(Kn[1], Q[qn][1], Sn); EX(Sc, 6); EX(Sc, 7); CV(Pc, Sc, 0); CV(Pc, Sc, 1); PIN(Sc); PIN(Pc[0]); SBAR(); \
        O[bp] = MFMA(Vp[1], BC(Pp[1]), O[bp]); EX(Sc, 8); EX(Sc, 9); CV(Pc, Sc, 2); CV(Pc, Sc, 3); PIN(Sc); PIN(Pc[0]); SBAR(); \
        L = MFMA16(sel[bp], BC(Pp[0]), L); EX(Sc, 10); EX(Sc, 11); EX(Sc, 12); CV(Pc, Sc, 4); PIN(Sc); PIN(Pc[1]); SBAR(); \
        L = MFMA16(sel[bp], BC(Pp[1]), L); EX(Sc, 13); EX(Sc, 14); EX(Sc, 15); CV(Pc, Sc, 5); CV(Pc, Sc, 6); CV(Pc, Sc, 7); \
        PIN(Pc[1]); SBAR();                                                                                     \
    } while (0)
    for (int t = 0; t < NT; t += 2) {
        const int n2 = t + 2 < NT ? t + 2 : t, n3 = t + 3 < NT ? t + 3 : t;
        STEP(SA, SB, PA, PB, Ka, 1, Vb, 3);
        if (t) { Vb[0] = kv[(t + 1) * 256 + 128]; Vb[1] = kv[(t + 1) * 256 + 192]; }
        STEP(SB, SA, PB, PA, Ka, 2, Va, 0);
        STEP(SA, SB, PA, PB, Ka, 3, Va, 1);
        Ka[0] = kv[n2 * 256]; Ka[1] = kv[n2 * 256 + 64];
        STEP(SB, SA, PB, PA, Kb, 0, Va, 2);
        STEP(SA, SB, PA, PB, Kb, 1, Va, 3);
        Va[0] = kv[n2 * 256 + 128]; Va[1] = kv[n2 * 256 + 192];
        STEP(SB, SA, PB, PA, Kb, 2, Vb, 0);
        STEP(SA, SB, PA, PB, Kb, 3, Vb, 1);
        Kb[0] = kv[n3 * 256]; Kb[1] = kv[n3 * 256 + 64];
        STEP(SB, SA, PB, PA, Ka, 0, Vb, 2);
    }
    O[3] = MFMA(Vb[0], BC(PB[0]), O[3]);
    O[3] = MFMA(Vb[1], BC(PB[1]), O[3]);
    L = MFMA16(sel[3], BC(PB[0]), L);
    L = MFMA16(sel[3], BC(PB[1]), L);
#undef STEP
    LDS_AS f32x4* ol = (LDS_AS f32x4*)((lds_ptr)smem + wid * 16384) + lane;
    LDS_AS f32x2* ml = (LDS_AS f32x2*)((lds_ptr)smem + NW * 16384 + wid * 2048) + lane;
    bool ok = true;
#pragma unroll
    for (int i = 0; i < 4; ++i) {
        ok = ok && L[i] >= 0x1p-40f && L[i] <= 0x1p40f;
        float a = 0.f;
#pragma unroll
        for (int r = 0; r < 16; r += 2) a = __builtin_fmaxf(__builtin_fmaxf(__builtin_fabsf(O[i][r]), __builtin_fabsf(O[i][r + 1])), a);
        float n = 0.f;
#pragma unroll
        for (int r = 0; r < 16; ++r) n += O[i][r] * 0.f;
        ok = ok && a < INFINITY && n == 0.f;
    }
    if (__builtin_expect(__all(ok), 1)) {
#pragma unroll
        for (int i = 0; i < 4; ++i) {
#pragma unroll
            for (int c = 0; c < 4; ++c) ol[(i * 4 + c) * 64] = (f32x4){O[i][4 * c], O[i][4 * c + 1], O[i][4 * c + 2], O[i][4 * c + 3]};
            ml[i * 64] = (f32x2){mref, L[i]};
        }
    } else {
#pragma unroll 1
        for (int i = 0; i < 4; ++i) {
            const bf16x8 q0 = qp[i * 64 * 64], q1 = qp[i * 64 * 64 + 64];
            float m = -INFINITY;
#pragma unroll 1
            for (int t = 0; t < NT; ++t) {
                f32x16 s = MFMA(kv[t * 256], q0, (f32x16){});
                s = MFMA(kv[t * 256 + 64], q1, s);
#pragma unroll
                for (int r = 0; r < 16; ++r) m = __builtin_fmaxf(m, s[r]);
            }
            m = xhalf_max(m);
            f32x16 o = {};
            float l = 0.f;
#pragma unroll 1
            for (int t = 0; t < NT; ++t) {
                f32x16 s = MFMA(kv[t * 256], q0, (f32x16){});
                s = MFMA(kv[t * 256 + 64], q1, s);
#pragma unroll
                for (int r = 0; r < 16; ++r) s[r] = __builtin_amdgcn_exp2f(s[r] - m);
                const u32x4 p0 = pack8n(s, 0), p1 = pack8n(s, 1);
#pragma unroll
                for (int e = 0; e < 4; ++e) l += bflo(p0[e]) + bfhi(p0[e]) + bflo(p1[e]) + bfhi(p1[e]);
                o = MFMA(kv[t * 256 + 128], BC(p0), o);
                o = MFMA(kv[t * 256 + 192], BC(p1), o);
            }
            l = xhalf_sum(l);
#pragma unroll
            for (int c = 0; c < 4; ++c) ol[(i * 4 + c) * 64] = (f32x4){o[4 * c], o[4 * c + 1], o[4 * c + 2], o[4 * c + 3]};
            ml[i * 64] = (f32x2){m, l};
        }
    }
    LG_BAR();
#pragma unroll
    for (int bi = 0; bi < 4 / KS; ++bi) {
        const int blk = ks * (4 / KS) + bi;
        f32x2 mlv[KS];
        float M = -INFINITY;
#pragma unroll
        for (int k = 0; k < KS; ++k) {
            mlv[k] = *((const LDS_AS f32x2*)((lds_ptr)smem + NW * 16384 + (2 * k + qg) * 2048) + blk * 64 + lane);
            M = __builtin_fmaxf(M, mlv[k][0]);
        }
        float l = 0.f, w[KS];
#pragma unroll
        for (int k = 0; k < KS; ++k) { w[k] = __builtin_amdgcn_exp2f(mlv[k][0] - M); l += w[k] * mlv[k][1]; }
        const int mblk = mblk0 + blk;
        const float g = *(const float*)(ws + WS_G + (size_t)(mblk * 32 + (lane & 31)) * 8);
        const float sc = g / l;
        f32x16 o = {};
#pragma unroll
        for (int k = 0; k < KS; ++k) {
            const float wk = w[k] * sc;
#pragma unroll
            for (int c = 0; c < 4; ++c) {
                const f32x4 v = *((const LDS_AS f32x4*)((lds_ptr)smem + (2 * k + qg) * 16384) + (blk * 4 + c) * 64 + lane);
#pragma unroll
                for (int e = 0; e < 4; ++e) o[4 * c + e] = __builtin_fmaf(v[e], wk, o[4 * c + e]);
            }
        }
#pragma unroll
        for (int s = 0; s < 2; ++s)
            *(u32x4*)(ws + WS_A4 + ((size_t)(mblk * KB4 + 224 + 2 * head + s) * 64 + lane) * 16) = pack8n(o, s);
    }
#undef EX
#undef CV
}

__global__ __launch_bounds__(512, 2) void k_gemm2(const u16* __restrict__ x, const u16* __restrict__ w2,
                                                  const u16* __restrict__ w3, char* __restrict__ ws) {
    extern __shared__ __attribute__((aligned(16))) char smem[];
    const int b = blockIdx.x, tid = threadIdx.x, lane = tid & 63, wid = rfl(tid >> 6);
    if (b >= 128) {
        const int c = b - 128, cc = c >> 1, kh = c & 1;
        const u16* p = w3 + (size_t)(kh * 512 + (tid >> 3)) * F_ + cc * 64 + (tid & 7) * 8;
        u32x4 v[8];
#pragma unroll
        for (int i = 0; i < 8; ++i) v[i] = *(const u32x4*)(p + (size_t)i * 64 * F_);
        float s[8] = {};
#pragma unroll
        for (int i = 0; i < 8; ++i)
#pragma unroll
            for (int e = 0; e < 4; ++e) { s[2 * e] += bflo(v[i][e]); s[2 * e + 1] += bfhi(v[i][e]); }
#pragma unroll
        for (int e = 0; e < 8; ++e) {
            s[e] += __shfl_xor(s[e], 8); s[e] += __shfl_xor(s[e], 16); s[e] += __shfl_xor(s[e], 32);
        }
        LDS_AS float* l = (LDS_AS float*)(lds_ptr)smem;
        if (lane < 8)
#pragma unroll
            for (int e = 0; e < 8; ++e) l[wid * 64 + lane * 8 + e] = s[e];
        LG_BAR();
        if (tid < 64) {
            float t = 0.f;
#pragma unroll
            for (int w = 0; w < 8; ++w) t += l[w * 64 + tid];
            ((float*)(ws + WS_CS3))[kh * F_ + cc * 64 + tid] = t;
        }
        return;
    }
    const int xcd = b & 7, jb = b >> 3, mt = 4 * (xcd >> 1) + (jb & 3), nt = 4 * (xcd & 1) + (jb >> 2);
    const int j = wid & 3, grp = wid >> 2, wm = j & 1, wn = j >> 1, h = lane >> 5, r32 = lane & 31;
    const int col0 = nt * 128 + grp * 64 + wn * 32;
    u32x4 xr[2][2];
#pragma unroll
    for (int mb = 0; mb < 2; ++mb)
#pragma unroll
        for (int s = 0; s < 2; ++s)
            xr[mb][s] = *(const u32x4*)(x + (size_t)(mt * 128 + wm * 64 + mb * 32 + r32) * D_ + col0 + 16 * s + 8 * h);
    f32x16 res[2];
    const char* wb = (const char*)(w2 + nt * 128);
    kloopB(smem, ws + WS_HS + (size_t)mt * 4 * 65536, 65536, 2048, [=](int t) { return wb + (size_t)t * 64 * 2048; }, 16, res);
    LDS_AS f32x2* st = (LDS_AS f32x2*)((lds_ptr)smem + 65536);
#pragma unroll
    for (int mb = 0; mb < 2; ++mb) {
        float s1 = 0.f, s2 = 0.f;
#pragma unroll
        for (int s = 0; s < 2; ++s) {
            auto a = __builtin_amdgcn_permlane32_swap(xr[mb][s][0], xr[mb][s][2], false, false);
            auto c = __builtin_amdgcn_permlane32_swap(xr[mb][s][1], xr[mb][s][3], false, false);
            const unsigned xx[4] = {a[0], c[0], a[1], c[1]};
            u32x4 o;
#pragma unroll
            for (int e = 0; e < 4; ++e) {
                o[e] = cvtpk(res[mb][8 * s + 2 * e] + bflo(xx[e]), res[mb][8 * s + 2 * e + 1] + bfhi(xx[e]));
                const float lo = bflo(o[e]), hi = bfhi(o[e]);
                s1 += lo + hi;
                s2 = __builtin_fmaf(lo, lo, __builtin_fmaf(hi, hi, s2));
            }
            *(u32x4*)(ws + WS_X1 + ((size_t)((mt * 4 + wm * 2 + mb) * 64 + (col0 >> 4) + s) * 64 + lane) * 16) = o;
        }
        s1 = xhalf_sum(s1);
        s2 = xhalf_sum(s2);
        if (h == 0) st[(grp * 2 + wn) * 128 + wm * 64 + mb * 32 + r32] = (f32x2){s1, s2};
    }
    LG_BAR();
    if (tid < 128) {
        const f32x2 t = st[tid] + st[128 + tid] + st[256 + tid] + st[384 + tid];
        *(f32x2*)(ws + WS_ST + ((size_t)nt * S_ + mt * 128 + tid) * 8) = t;
    }
}

__global__ __launch_bounds__(512, 2) void k_gemm3(const u16* __restrict__ w3, char* __restrict__ ws) {
    extern __shared__ __attribute__((aligned(16))) char smem[];
    const int b = blockIdx.x, xcd = b & 7, jb = b >> 3, mt = 4 * (xcd & 1) + (jb & 3), nt = 7 * (xcd >> 1) + (jb >> 2);
    const int tid = threadIdx.x, lane = tid & 63, wid = rfl(tid >> 6), wm = wid & 3, wn = wid >> 2;
    const int h = lane >> 5, r32 = lane & 31;
    f32x2 sp[2][4];
    float omg[2];
#pragma unroll
    for (int mb = 0; mb < 2; ++mb) {
        const int row = mt * 256 + wm * 64 + mb * 32 + r32;
#pragma unroll
        for (int i = 0; i < 4; ++i) sp[mb][i] = *(const f32x2*)(ws + WS_ST + ((size_t)(4 * h + i) * S_ + row) * 8);
        omg[mb] = *(const float*)(ws + WS_G + (size_t)row * 8 + 4);
    }
    f32x16 acc[2][2] = {};
    f32x2 d0, d1, d2;
    const char* wb = (const char*)(w3 + nt * 128);
    kloopA<2>(smem, ws + WS_X1 + (size_t)mt * 8 * 65536, 65536, wb, F_ * 2, wb + 128, F_ * 2, 16, acc, d0, d1, d2);
    float mean[2], rstd[2];
#pragma unroll
    for (int mb = 0; mb < 2; ++mb) {
        const f32x2 t = sp[mb][0] + sp[mb][1] + sp[mb][2] + sp[mb][3];
        const float s1 = xhalf_sum(t[0]), s2 = xhalf_sum(t[1]);
        mean[mb] = s1 * (1.0f / D_);
        rstd[mb] = __builtin_amdgcn_rsqf(__builtin_fmaxf(s2 * (1.0f / D_) - mean[mb] * mean[mb], 0.f) + 1e-5f);
    }
#pragma unroll
    for (int nb = 0; nb < 2; ++nb) {
        const int col0 = nt * 128 + nb * 64 + wn * 32;
        float c16[16];
#pragma unroll
        for (int i = 0; i < 4; ++i) {
            const float* cp = (const float*)(ws + WS_CS3) + col0 + 8 * i + 4 * h;
            const f32x4 c = *(const f32x4*)cp + *(const f32x4*)(cp + F_);
#pragma unroll
            for (int e = 0; e < 4; ++e) c16[4 * i + e] = c[e];
        }
#pragma unroll
        for (int mb = 0; mb < 2; ++mb) {
            f32x16 hv;
#pragma unroll
            for (int r = 0; r < 16; ++r) hv[r] = gelu_t(__builtin_fmaf(-mean[mb], c16[r], acc[nb][mb][r]) * rstd[mb]);
#pragma unroll
            for (int s = 0; s < 2; ++s)
                *(u32x4*)(ws + WS_A4 + ((size_t)((mt * 8 + wm * 2 + mb) * KB4 + (col0 >> 4) + s) * 64 + lane) * 16) =
                    pack8(hv, s, omg[mb]);
        }
    }
}

__global__ __launch_bounds__(512, 2) void k_gemm4(const u16* __restrict__ w4, const u16* __restrict__ wo,
                                                  char* __restrict__ ws, u16* __restrict__ out) {
    extern __shared__ __attribute__((aligned(16))) char smem[];
    const int b = blockIdx.x, xcd = b & 7, jb = b >> 3;
    const int ks = xcd & 1, mt = 8 * ((xcd >> 1) & 1) + (jb & 7), nt = 4 * (xcd >> 2) + (jb >> 3);
    const int tid = threadIdx.x, lane = tid & 63, wid = rfl(tid >> 6);
    const int j = wid & 3, grp = wid >> 2, wm = j & 1, wn = j >> 1, h = lane >> 5, r32 = lane & 31;
    const int col0 = nt * 128 + grp * 64 + wn * 32, mblk = mt * 4 + wm * 2 + ks;
    u32x4 xr[2];
#pragma unroll
    for (int s = 0; s < 2; ++s) xr[s] = *(const u32x4*)(ws + WS_X1 + ((size_t)(mblk * 64 + (col0 >> 4) + s) * 64 + lane) * 16);
    const float omg = *(const float*)(ws + WS_G + (size_t)(mblk * 32 + r32) * 8 + 4);
    f32x16 res[2];
    const char* w4b = (const char*)(w4 + nt * 128);
    const char* wob = (const char*)(wo + nt * 128);
    const int t0 = ks * 36;
    kloopB(smem, ws + WS_A4 + (size_t)mt * 4 * KB4 * 1024 + (size_t)t0 * 4096, KB4 * 1024, 2048,
           [=](int t) { const int tt = t0 + t; return tt < 56 ? w4b + (size_t)tt * 64 * 2048 : wob + (size_t)(tt - 56) * 64 * 2048; },
           36, res);
    const int tile = mt * 8 + nt;
    {
        char* dst = ws + WS_EX + (size_t)(tile * 2 + ks) * 32768 + wid * 4096 + lane * 16;
        const f32x16& a = ks ? res[0] : res[1];
#pragma unroll
        for (int i = 0; i < 4; ++i) {
            const f32x4 v = {a[4 * i], a[4 * i + 1], a[4 * i + 2], a[4 * i + 3]};
            asm volatile("global_store_dwordx4 %0, %1, off sc1\n\ts_nop 1" ::"v"(dst + i * 1024), "v"(v) : "memory");
        }
    }
    asm volatile("s_waitcnt vmcnt(0)\n\ts_barrier" ::: "memory");
    gu32* flags = (gu32*)(ws + WS_FLAG);
    if (wid == 0) {
        if (lane == 0) __hip_atomic_store(flags + tile * 2 + ks, 1u, __ATOMIC_RELAXED, __HIP_MEMORY_SCOPE_AGENT);
        while (__hip_atomic_load(flags + tile * 2 + (ks ^ 1), __ATOMIC_RELAXED, __HIP_MEMORY_SCOPE_AGENT) == 0u)
            __builtin_amdgcn_s_sleep(1);
    }
    BAR();
    f32x4 pv[4];
    {
        const char* src = ws + WS_EX + (size_t)(tile * 2 + (ks ^ 1)) * 32768 + wid * 4096 + lane * 16;
        asm volatile(
            "global_load_dwordx4 %0, %4, off sc1\n\tglobal_load_dwordx4 %1, %4, off offset:1024 sc1\n\t"
            "global_load_dwordx4 %2, %4, off offset:2048 sc1\n\tglobal_load_dwordx4 %3, %4, off offset:3072 sc1\n\t"
            "s_waitcnt vmcnt(0)"
            : "=&v"(pv[0]), "=&v"(pv[1]), "=&v"(pv[2]), "=&v"(pv[3])
            : "v"(src)
            : "memory");
    }
    const f32x16& a = ks ? res[1] : res[0];
#pragma unroll
    for (int s = 0; s < 2; ++s) {
        unsigned o[4];
#pragma unroll
        for (int e = 0; e < 4; ++e) {
            const int r = 8 * s + 2 * e;
            o[e] = cvtpk(__builtin_fmaf(bflo(xr[s][e]), omg, a[r] + pv[r >> 2][r & 3]),
                         __builtin_fmaf(bfhi(xr[s][e]), omg, a[r + 1] + pv[(r + 1) >> 2][(r + 1) & 3]));
        }
        auto u = __builtin_amdgcn_permlane32_swap(o[0], o[2], false, false);
        auto v = __builtin_amdgcn_permlane32_swap(o[1], o[3], false, false);
        *(u32x4*)(out + (size_t)(mblk * 32 + r32) * D_ + col0 + 16 * s + 8 * h) = (u32x4){u[0], v[0], u[1], v[1]};
    }
}

constexpr int ATT_NW = 4;

extern "C" void kernel_launch(void* const* d_in, const int* in_sizes, int n_in, void* d_out, int out_size, void* d_ws,
                              size_t ws_size, hipStream_t stream) {
    const u16* x = (const u16*)d_in[0];
    const u16* wqkv = (const u16*)d_in[1];
    const u16* wo = (const u16*)d_in[2];
    const u16* w1 = (const u16*)d_in[4];
    const u16* w2 = (const u16*)d_in[5];
    const u16* w3 = (const u16*)d_in[6];
    const u16* w4 = (const u16*)d_in[7];
    const u16* gw = (const u16*)d_in[8];
    char* ws = (char*)d_ws;
    constexpr int LDS_ATT = ATT_NW * (16384 + 2048);
    (void)hipFuncSetAttribute((const void*)k_gemm1, hipFuncAttributeMaxDynamicSharedMemorySize, 3 * STG_A);
    (void)hipFuncSetAttribute((const void*)k_gemm3, hipFuncAttributeMaxDynamicSharedMemorySize, 3 * STG_A);
    (void)hipFuncSetAttribute((const void*)k_gemm2, hipFuncAttributeMaxDynamicSharedMemorySize, 4 * STG_B);
    (void)hipFuncSetAttribute((const void*)k_gemm4, hipFuncAttributeMaxDynamicSharedMemorySize, 4 * STG_B);
    (void)hipFuncSetAttribute((const void*)k_attn<ATT_NW>, hipFuncAttributeMaxDynamicSharedMemorySize, LDS_ATT);
    k_gemm1<<<256, 512, 3 * STG_A, stream>>>(x, wqkv, w1, gw, ws);
    k_attn<ATT_NW><<<256, ATT_NW * 64, LDS_ATT, stream>>>(ws);
    k_gemm2<<<240, 512, 4 * STG_B, stream>>>(x, w2, w3, ws);
    k_gemm3<<<224, 512, 3 * STG_A, stream>>>(w3, ws);
    k_gemm4<<<256, 512, 4 * STG_B, stream>>>(w4, wo, ws, (u16*)d_out);
}
```

```cpp
#include <hip/hip_runtime.h>
#include <hip/hip_bf16.h>
#include <stdint.h>

typedef unsigned short u16;
typedef unsigned long long u64;
typedef short bf16x8 __attribute__((ext_vector_type(8)));
typedef short v4i16_t __attribute__((ext_vector_type(4)));
typedef __bf16 bf2_t __attribute__((ext_vector_type(2)));
typedef float f32x16 __attribute__((ext_vector_type(16)));
typedef float f32x4 __attribute__((ext_vector_type(4)));
typedef float f32x2 __attribute__((ext_vector_type(2)));
typedef unsigned u32x4 __attribute__((ext_vector_type(4)));
typedef unsigned u32x2 __attribute__((ext_vector_type(2)));
typedef __attribute__((address_space(3))) char* lds_ptr;
typedef __amdgpu_buffer_rsrc_t rsrc_t;
#define DEV __device__ __forceinline__
#define LDS_AS __attribute__((address_space(3)))
#define SBAR() __builtin_amdgcn_sched_barrier(0)
#define PIN(x) asm volatile("" : "+v"(x))
#define MFMA32(a, b, c) __builtin_amdgcn_mfma_f32_32x32x16_bf16(a, b, c, 0, 0, 0)
#define MFMA16(a, b, c) __builtin_amdgcn_mfma_f32_16x16x32_bf16(a, b, c, 0, 0, 0)
#define BAR() asm volatile("s_barrier" ::: "memory")
#define WAIT_BAR(N) asm volatile("s_waitcnt vmcnt(" #N ") lgkmcnt(0)\n\ts_barrier" ::: "memory")
#define LG_BAR() asm volatile("s_waitcnt lgkmcnt(0)\n\ts_barrier" ::: "memory")
#define DRAIN() asm volatile("s_waitcnt vmcnt(0)" ::: "memory")
#define BC8(x) __builtin_bit_cast(bf16x8, x)

constexpr int S_ = 2048, D_ = 1024, F_ = 3584;
constexpr unsigned KB = 1024, MB = 1u << 20;
constexpr unsigned WS_EPOCH = 0;
constexpr unsigned WS_F1 = 4 * KB;
constexpr unsigned WS_FH = 16 * KB;
constexpr unsigned WS_F2 = 24 * KB;
constexpr unsigned WS_FO = 40 * KB;
constexpr unsigned WS_F3 = 56 * KB;
constexpr unsigned WS_FX = 72 * KB;
constexpr unsigned WS_RS = 128 * KB;
constexpr unsigned WS_G = 160 * KB;
constexpr unsigned WS_ST = 256 * KB;
constexpr unsigned WS_Q = 1 * MB;
constexpr unsigned WS_KV = 5 * MB;
constexpr unsigned WS_HS = 13 * MB;
constexpr unsigned WS_X1 = 17 * MB;
constexpr unsigned WS_A4 = 21 * MB;
constexpr unsigned WS_EX = 40 * MB;
constexpr int LDS_BYTES = 160 * 1024;
constexpr unsigned SPIN_CAP = 1u << 18;

#define STAMP(k)

DEV unsigned cvtpk(float lo, float hi) {
    const f32x2 v = {lo, hi};
    return __builtin_bit_cast(unsigned, __builtin_convertvector(v, bf2_t));
}
DEV float bflo(unsigned u) { return __uint_as_float(u << 16); }
DEV float bfhi(unsigned u) { return __uint_as_float(u & 0xffff0000u); }
DEV float rbf(float f) { return bflo(cvtpk(f, f)); }
DEV float dot2(unsigned a, unsigned b, float c) {
    return __builtin_amdgcn_fdot2_f32_bf16(__builtin_bit_cast(bf2_t, a), __builtin_bit_cast(bf2_t, b), c, false);
}
DEV u32x4 pack44(const f32x4& a, const f32x4& b) {
    return (u32x4){cvtpk(a[0], a[1]), cvtpk(a[2], a[3]), cvtpk(b[0], b[1]), cvtpk(b[2], b[3])};
}
DEV float sum32(float v) {
    auto r = __builtin_amdgcn_permlane32_swap(__float_as_uint(v), __float_as_uint(v), false, false);
    return __uint_as_float(r[0]) + __uint_as_float(r[1]);
}
DEV float sum16(float v) {
    auto r = __builtin_amdgcn_permlane16_swap(__float_as_uint(v), __float_as_uint(v), false, false);
    return __uint_as_float(r[0]) + __uint_as_float(r[1]);
}
DEV float max32(float v) {
    auto r = __builtin_amdgcn_permlane32_swap(__float_as_uint(v), __float_as_uint(v), false, false);
    return __builtin_fmaxf(__uint_as_float(r[0]), __uint_as_float(r[1]));
}
template <int CTRL>
DEV float dpp_add(float v) {
    return v + __uint_as_float(__builtin_amdgcn_update_dpp(0u, __float_as_uint(v), CTRL, 0xf, 0xf, true));
}
DEV float wave_sum(float v) {
    v = dpp_add<0xB1>(v);
    v = dpp_add<0x4E>(v);
    v = dpp_add<0x141>(v);
    v = dpp_add<0x140>(v);
    return sum32(sum16(v));
}
DEV bf16x8 ld128(lds_ptr p) { return *(const LDS_AS bf16x8*)p; }
DEV bf16x8 ldtr(lds_ptr p) {
    v4i16_t a = __builtin_amdgcn_ds_read_tr16_b64_v4i16((LDS_AS v4i16_t*)p);
    v4i16_t b = __builtin_amdgcn_ds_read_tr16_b64_v4i16((LDS_AS v4i16_t*)(p + 2048));
    return (bf16x8){a[0], a[1], a[2], a[3], b[0], b[1], b[2], b[3]};
}
DEV int rfl(int v) { return __builtin_amdgcn_readfirstlane(v); }
DEV int fresh_lane() {
    int l = __builtin_amdgcn_mbcnt_hi(~0u, __builtin_amdgcn_mbcnt_lo(~0u, 0u));
    PIN(l);
    return l;
}
DEV u32x4 ldw4(rsrc_t r, unsigned v, unsigned s) { return __builtin_amdgcn_raw_buffer_load_b128(r, v, s, 16); }
DEV u32x2 ldw2(rsrc_t r, unsigned v, unsigned s) { return __builtin_amdgcn_raw_buffer_load_b64(r, v, s, 16); }
DEV void stw4(rsrc_t r, unsigned v, unsigned s, u32x4 x) {
    __builtin_amdgcn_raw_buffer_store_b128(x, r, v, s, 16);
    asm volatile("s_nop 1" ::"v"(x) : "memory");
}
DEV void stw2(rsrc_t r, unsigned v, unsigned s, u32x2 x) { __builtin_amdgcn_raw_buffer_store_b64(x, r, v, s, 16); }
#define GLDS(dst, voff, sptr, IMM, SC)                                                                        \
    asm volatile("s_mov_b32 m0, %0\n\ts_nop 0\n\tglobal_load_lds_dwordx4 %1, %2 offset:" #IMM SC ::"s"(dst), "v"(voff), \
                 "s"(sptr) : "memory")
#define IF_EQ(t, K) "s_cmp_lg_u32 " t ", " #K "\n\ts_cbranch_scc1 .Lskip%=\n\t"
#define END_IF "\n.Lskip%=:"
DEV void post_flag(rsrc_t r, unsigned off, u32x2 tag, int lane) {
    DRAIN();
    if (lane == 0) stw2(r, 0, off, tag);
}
DEV void wait_flags(rsrc_t r, unsigned voff, u32x2 tag) {
#pragma unroll 1
    for (unsigned n = 0; n < SPIN_CAP; ++n) {
        asm volatile("" ::: "memory");
        const u32x2 v = ldw2(r, voff, 0);
        if (__all(v[0] == tag[0] && v[1] == tag[1])) break;
        __builtin_amdgcn_s_sleep(2);
    }
    asm volatile("" ::: "memory");
}
DEV f32x2 gelu2(f32x2 v, float a, float b, float k) {
    const f32x2 y = v * __builtin_elementwise_fma(v * v, (f32x2){b, b}, (f32x2){a, a});
    const f32x2 e = {__builtin_amdgcn_exp2f(y[0]), __builtin_amdgcn_exp2f(y[1])};
    const f32x2 d = __builtin_elementwise_fma(e, (f32x2){k, k}, (f32x2){k, k});
    return v * (f32x2){__builtin_amdgcn_rcpf(d[0]), __builtin_amdgcn_rcpf(d[1])};
}
constexpr float GC0 = -2.0f * 0.7978845608028654f * 1.4426950408889634f, GC1 = GC0 * 0.044715f;
DEV u32x4 ln_gelu_chunk(const f32x4& p, const f32x4& q, const f32x4& cp, const f32x4& cq, float mean, float a, float b, float k) {
    const f32x2 nm = {-mean, -mean};
    const f32x2 r0 = gelu2(__builtin_elementwise_fma(nm, (f32x2){cp[0], cp[1]}, (f32x2){p[0], p[1]}), a, b, k);
    const f32x2 r1 = gelu2(__builtin_elementwise_fma(nm, (f32x2){cp[2], cp[3]}, (f32x2){p[2], p[3]}), a, b, k);
    const f32x2 r2 = gelu2(__builtin_elementwise_fma(nm, (f32x2){cq[0], cq[1]}, (f32x2){q[0], q[1]}), a, b, k);
    const f32x2 r3 = gelu2(__builtin_elementwise_fma(nm, (f32x2){cq[2], cq[3]}, (f32x2){q[2], q[3]}), a, b, k);
    return (u32x4){cvtpk(r0[0], r0[1]), cvtpk(r1[0], r1[1]), cvtpk(r2[0], r2[1]), cvtpk(r3[0], r3[1])};
}

constexpr unsigned STG_A = 49152, CSL = 3 * STG_A;
struct LoadA {
    const char *xs, *w0, *w1;
    unsigned xv[4], wv0, wv1, xstep, wstep0, wstep1, dx, dw;
};
template <bool FO>
DEV void setupA(LoadA& L, int lane, int wid, unsigned lds0, const char* xs, const char* w0, unsigned ld0, const char* w1,
                unsigned ld1) {
    L.xs = xs; L.w0 = w0; L.w1 = w1;
    if (FO) {
        L.xv[0] = L.xv[1] = (2 * wid) * 32768 + lane * 16;
        L.xv[2] = L.xv[3] = L.xv[0] + 32768 - 2048;
    } else {
#pragma unroll
        for (int i = 0; i < 4; ++i) {
            const int row = 32 * wid + 8 * i + (lane >> 3);
            L.xv[i] = row * 2048 + (((lane & 7) ^ ((row >> 1) & 7)) << 4) - i * 1024;
        }
    }
    const int ir = 8 * wid + (lane >> 3);
    const int sr = FO ? ir : ((ir & 32) | ((ir & 12) << 1) | ((ir & 16) >> 2) | (ir & 3));
    const unsigned wch = ((lane & 7) ^ (((ir >> 1) & 3) << 1)) << 4;
    L.wv0 = sr * ld0 + wch; L.wv1 = sr * ld1 + wch;
    L.xstep = FO ? 2048u : 128u; L.wstep0 = 64 * ld0; L.wstep1 = 64 * ld1;
    L.dx = lds0 + wid * 4096; L.dw = lds0 + 32768 + wid * 1024;
}
#define A_X(L, i, so, FO)                                                                  \
    do {                                                                                   \
        if (FO) { GLDS_I(L.dx + (so), L.xv[i], L.xs, i, " sc1"); }                         \
        else { GLDS_I(L.dx + (so), L.xv[i], L.xs, i, ""); }                                \
    } while (0)
#define GLDS_I(dst, v, s, i, SC)                                                           \
    if (i == 0) GLDS(dst, v, s, 0, SC); else if (i == 1) GLDS(dst, v, s, 1024, SC);        \
    else if (i == 2) GLDS(dst, v, s, 2048, SC); else GLDS(dst, v, s, 3072, SC)
#define A_W0(L, so) GLDS(L.dw + (so), L.wv0, L.w0, 0, "")
#define A_W1(L, so) GLDS(L.dw + 8192 + (so), L.wv1, L.w1, 0, "")

template <bool FO, int mode, class Hook>
DEV void kloopA(char* smem, LoadA& L, int lane, int wid, f32x4 (&acc)[4][4], Hook hook) {
    const int wm = wid & 3, wn = wid >> 2, c = lane & 15, g = lane >> 4, q = c >> 2;
    const int sx = mode == 2 ? (wm & 1) : 0, nx = mode == 2 ? (wm & 2) : 0;
    lds_ptr xb[2], wb[4];
#pragma unroll
    for (int s = 0; s < 2; ++s) {
        const int sa = s ^ sx;
        xb[s] = (lds_ptr)smem + (FO ? (8 * wm + sa) * 1024 + lane * 16 : (64 * wm + c) * 128 + (((4 * sa + g) ^ ((c >> 1) & 7)) << 4));
    }
#pragma unroll
    for (int nb = 0; nb < 4; ++nb)
        wb[nb] = (lds_ptr)smem + 32768 + wn * 8192 + sx * 4096 + (4 * g + q) * 128 + (((nb ^ nx) ^ ((2 * g + (q >> 1)) & 3)) << 5) + (lane & 3) * 8;
    const int sstep = 4096 - 8192 * sx;
    lds_ptr wb1[4];
#pragma unroll
    for (int nb = 0; nb < 4; ++nb) wb1[nb] = wb[nb] + sstep;
    float cs0 = 0.f, cs1 = 0.f, cs2 = 0.f, cs3 = 0.f;
    unsigned so2 = 2 * STG_A;
    int st = 0;
#pragma unroll 1
    for (int t = 0; t < 16; ++t) {
        bf16x8 xf[4][2], wf[4][2];
        const bool more = t < 14;
        SBAR();
        if (more) { A_X(L, 0, so2, FO); A_W0(L, so2); }
        SBAR();
        wf[0][0] = ldtr(wb[0]); xf[0][0] = ld128(xb[0]); wf[1][0] = ldtr(wb[1]); xf[1][0] = ld128(xb[0] + 2048);
        wf[2][0] = ldtr(wb[2]); xf[2][0] = ld128(xb[0] + 4096); wf[3][0] = ldtr(wb[3]); xf[3][0] = ld128(xb[0] + 6144);
        SBAR();
        if (more) { A_X(L, 1, so2, FO); A_X(L, 2, so2, FO); }
        SBAR();
        wf[0][1] = ldtr(wb1[0]); xf[0][1] = ld128(xb[1]); wf[1][1] = ldtr(wb1[1]); xf[1][1] = ld128(xb[1] + 2048);
        wf[2][1] = ldtr(wb1[2]); xf[2][1] = ld128(xb[1] + 4096); wf[3][1] = ldtr(wb1[3]); xf[3][1] = ld128(xb[1] + 6144);
        SBAR();
        if (more) {
            A_X(L, 3, so2, FO); A_W1(L, so2);
            L.xs += L.xstep; L.w0 += L.wstep0; L.w1 += L.wstep1;
            WAIT_BAR(6);
        } else if (t == 14) {
            WAIT_BAR(0);
        } else {
            LG_BAR();
        }
        hook(t);
        SBAR();
        const int adv = st == 2 ? -2 * (int)STG_A : (int)STG_A;
        st = st == 2 ? 0 : st + 1;
        so2 = so2 == 2 * STG_A ? 0 : so2 + STG_A;
        __builtin_amdgcn_s_setprio(1);
#define ADV(i)                                                                                       \
    do {                                                                                             \
        if ((i) >= 2 && (i) < 4) { xb[(i) & 1] += adv; PIN(xb[(i) & 1]); }                           \
        if ((i) >= 4 && (i) < 8) { wb[(i) & 3] += adv; PIN(wb[(i) & 3]); }                           \
        if ((i) >= 8 && (i) < 12) { wb1[(i) & 3] += adv; PIN(wb1[(i) & 3]); }                        \
        SBAR();                                                                                      \
    } while (0)
        if (mode == 0) {
#pragma unroll
            for (int i = 0; i < 32; ++i) {
                acc[i & 3][(i >> 2) & 3] = MFMA16(wf[i & 3][i >> 4], xf[(i >> 2) & 3][i >> 4], acc[i & 3][(i >> 2) & 3]);
                ADV(i);
            }
        } else if (mode == 1) {
#pragma unroll
            for (int i = 0; i < 32; ++i) {
                acc[i & 3][(i >> 2) & 3] = MFMA16(xf[(i >> 2) & 3][i >> 4], wf[i & 3][i >> 4], acc[i & 3][(i >> 2) & 3]);
                ADV(i);
            }
        } else {
            const u32x4 u0 = __builtin_bit_cast(u32x4, wf[0][0]), u1 = __builtin_bit_cast(u32x4, wf[1][0]);
            float tmp = 0.f;
#pragma unroll
            for (int i = 0; i < 32; ++i) {
                const unsigned u = (i & 16) ? u1[(i >> 2) & 3] : u0[(i >> 2) & 3];
                if (i == 31) { cs3 += tmp; PIN(cs3); SBAR(); }
                acc[i & 3][(i >> 2) & 3] = MFMA16(wf[i & 3][i >> 4], xf[(i >> 2) & 3][i >> 4], acc[i & 3][(i >> 2) & 3]);
                if ((i & 1) == 0) { tmp = (i & 2) ? bfhi(u) : bflo(u); PIN(tmp); }
                else if (i == 31) {}
                else if (i & 16) { if (i & 2) { cs3 += tmp; PIN(cs3); } else { cs2 += tmp; PIN(cs2); } }
                else { if (i & 2) { cs1 += tmp; PIN(cs1); } else { cs0 += tmp; PIN(cs0); } }
                ADV(i);
            }
        }
#undef ADV
        __builtin_amdgcn_s_setprio(0);
        SBAR();
        if (t != 15) BAR();
    }
    if (mode == 2) {
        const float a = sum32(sum16(cs0 + cs1)), b = sum32(sum16(cs2 + cs3));
        LDS_AS float* csl = (LDS_AS float*)((lds_ptr)smem + CSL) + wn * 128 + sx * 64 + nx * 16 + c;
        if (g == 0) { csl[0] = a; csl[16] = b; }
    }
    if (!wn) LG_BAR();
}
DEV void read_cs(char* smem, int lane, int wid, f32x4 (&cs)[4]) {
    const LDS_AS float* csl = (const LDS_AS float*)((lds_ptr)smem + CSL) + (wid >> 2) * 128 + (lane >> 4) * 4;
#pragma unroll
    for (int nb = 0; nb < 4; ++nb)
        cs[nb] = *(const LDS_AS f32x4*)(csl + (nb ^ (wid & 2)) * 16) + *(const LDS_AS f32x4*)(csl + 64 + (nb ^ (wid & 2)) * 16);
}

__global__ __launch_bounds__(512, 2) void k_mega(const u16* __restrict__ x, const u16* __restrict__ wqkv,
                                                 const u16* __restrict__ wo, const u16* __restrict__ w1,
                                                 const u16* __restrict__ w2, const u16* __restrict__ w3,
                                                 const u16* __restrict__ w4, const u16* __restrict__ gate_w,
                                                 char* __restrict__ ws, u16* __restrict__ out) {
    extern __shared__ __attribute__((aligned(16))) char smem[];
    const int b = blockIdx.x, xcd = b & 7, jb = b >> 3;
    const int wid = rfl(threadIdx.x >> 6);
    const unsigned lds0 = (unsigned)(uintptr_t)(lds_ptr)smem;
    const rsrc_t R = __builtin_amdgcn_make_buffer_rsrc(ws, 0, 0x7fffffff, 0x00020000);
    STAMP(0);
    u32x2 tag;
    {
        const int lane = fresh_lane(), c = lane & 15, g = lane >> 4, wm = wid & 3, wn = wid >> 2;
        const int mt = 4 * (xcd & 1) + (jb & 3), nt = 8 * (xcd >> 1) + (jb >> 2), jj = nt >> 1, qk = nt & 1;
        LoadA L;
        setupA<false>(L, lane, wid, lds0, (const char*)(x + (size_t)mt * 256 * D_),
                      qk ? (const char*)(wqkv + 64 * jj) : (const char*)(w1 + 64 * jj), qk ? 6144u : 2048u,
                      (const char*)(wqkv + (qk ? 1024 : 2048) + 64 * jj), 6144u);
        u32x2 ep;
        {
            const unsigned z = 0;
            asm volatile("global_load_dwordx2 %0, %1, %2 sc1" : "=v"(ep) : "v"(z), "s"(ws) : "memory");
        }
        A_X(L, 0, 0, false); A_W0(L, 0); A_X(L, 1, 0, false); A_X(L, 2, 0, false); A_X(L, 3, 0, false); A_W1(L, 0);
        L.xs += L.xstep; L.w0 += L.wstep0; L.w1 += L.wstep1;
        const int srow = mt * 256 + nt * 8 + wm * 2;
        u32x4 gx[4], gw[2];
        {
            const char* xr = (const char*)(x + (size_t)srow * D_);
            const unsigned vo = lane * 16;
            asm volatile(IF_EQ("%6", 1)
                "global_load_dwordx4 %0, %4, %5\n\tglobal_load_dwordx4 %1, %4, %5 offset:1024\n\t"
                "global_load_dwordx4 %2, %4, %5 offset:2048\n\tglobal_load_dwordx4 %3, %4, %5 offset:3072" END_IF
                : "=&v"(gx[0]), "=&v"(gx[1]), "=&v"(gx[2]), "=&v"(gx[3]) : "v"(vo), "s"(xr), "s"(wn) : "memory", "scc");
        }
        A_X(L, 0, STG_A, false); A_W0(L, STG_A); A_X(L, 1, STG_A, false); A_X(L, 2, STG_A, false); A_X(L, 3, STG_A, false);
        A_W1(L, STG_A);
        L.xs += L.xstep; L.w0 += L.wstep0; L.w1 += L.wstep1;
        asm volatile("s_waitcnt vmcnt(10)\n\t" IF_EQ("%5", 0) "s_waitcnt vmcnt(6)" END_IF "\n\ts_barrier\n\ts_waitcnt vmcnt(6)"
                     : "+v"(ep), "+v"(gx[0]), "+v"(gx[1]), "+v"(gx[2]), "+v"(gx[3]) : "s"(wn) : "memory", "scc");
        {
            const u64 e = ((u64)(unsigned)rfl(ep[1]) << 32 | (unsigned)rfl(ep[0])) + 1;
            tag = (u32x2){(unsigned)e, (unsigned)(e >> 32)};
        }
        STAMP(1);
        if (wn) {
            float s1[2] = {0.f, 0.f}, s2[2] = {0.f, 0.f};
#pragma unroll
            for (int i = 0; i < 4; ++i)
#pragma unroll
                for (int e = 0; e < 4; ++e) {
                    const unsigned u = gx[i][e];
                    s1[i >> 1] = dot2(u, 0x3F803F80u, s1[i >> 1]);
                    s2[i >> 1] = dot2(u, u, s2[i >> 1]);
                }
            auto p1 = __builtin_amdgcn_permlane32_swap(__float_as_uint(s1[0]), __float_as_uint(s1[1]), false, false);
            auto p2 = __builtin_amdgcn_permlane32_swap(__float_as_uint(s2[0]), __float_as_uint(s2[1]), false, false);
            float a1 = __uint_as_float(p1[0]) + __uint_as_float(p1[1]), a2 = __uint_as_float(p2[0]) + __uint_as_float(p2[1]);
            a1 = dpp_add<0xB1>(a1); a2 = dpp_add<0xB1>(a2); a1 = dpp_add<0x4E>(a1); a2 = dpp_add<0x4E>(a2);
            a1 = dpp_add<0x141>(a1); a2 = dpp_add<0x141>(a2); a1 = dpp_add<0x140>(a1); a2 = dpp_add<0x140>(a2);
            a1 = sum16(a1); a2 = sum16(a2);
            const float mean = a1 * (1.0f / D_);
            const float rstd = __builtin_amdgcn_rsqf(__builtin_fmaxf(a2 * (1.0f / D_) - mean * mean, 0.f) + 1e-5f);
            if ((lane & 30) == 0)
                stw2(R, (srow + (lane >> 5)) * 16 + (lane & 1) * 8, WS_RS,
                     (u32x2){__float_as_uint((lane & 1) ? rstd : mean), tag[0]});
            BAR();
        }
        const int rg = mt * 4 + wm;
        auto no_hook = [](int) {};
        auto load_gw = [&]() {
            gw[0] = *(const u32x4*)(gate_w + lane * 8);
            gw[1] = *(const u32x4*)(gate_w + 512 + lane * 8);
            SBAR();
        };
        unsigned fo;
        bool fp = lane == 0;
        if (qk) {
            f32x4 acc[4][4] = {};
            kloopA<false, 0>(smem, L, lane, wid, acc, no_hook);
            STAMP(2);
            load_gw();
            const float sc = wn ? 1.0f : 0.17677669529663687f * 1.4426950408889634f;
            const unsigned base = wn ? WS_KV + (2 * jj * 64 + 2 * rg) * 4096 : WS_Q + (2 * rg * 32 + 2 * jj) * 2048;
            const unsigned hstep = wn ? 64 * 4096 : 2048, bstep = wn ? 4096 : 65536, astep = wn ? 128 : 256;
            const unsigned vo = (g >> 1) * 1024 + ((g & 1) * 32 + (wn ? (c >> 3) * 16 + (c & 7) : c)) * 16;
#pragma unroll
            for (int mb = 0; mb < 4; ++mb)
#pragma unroll
                for (int hh = 0; hh < 2; ++hh)
                    stw4(R, vo, base + hh * hstep + (mb >> 1) * bstep + (mb & 1) * astep,
                         pack44(acc[2 * hh][mb] * sc, acc[2 * hh + 1][mb] * sc));
            fo = WS_F1 + ((wn * 16 + jj) * 32 + rg) * 8;
        } else if (wn) {
            f32x4 acc[4][4] = {};
            kloopA<false, 1>(smem, L, lane, wid, acc, no_hook);
            STAMP(2);
            load_gw();
            const unsigned base = WS_KV + (2 * jj * 64 + 2 * rg) * 4096 + 2048;
            const unsigned vo = (g >> 1) * 1024 + ((g & 1) * 32 + c) * 16;
#pragma unroll
            for (int i = 0; i < 2; ++i)
#pragma unroll
                for (int nb = 0; nb < 4; ++nb)
                    stw4(R, vo + (nb & 1) * 256, base + (nb >> 1) * 64 * 4096 + i * 4096, pack44(acc[nb][2 * i], acc[nb][2 * i + 1]));
            fo = WS_F1 + ((32 + jj) * 32 + rg) * 8;
        } else {
            f32x4 acc[4][4] = {};
            u32x4 rsv[4] = {};
            const unsigned rso = WS_RS + (mt * 256 + wm * 64 + c) * 16;
            kloopA<false, 2>(smem, L, lane, wid, acc, [&](int t) {
                asm volatile(IF_EQ("%6", 14)
                    "global_load_dwordx4 %0, %4, %5 sc1\n\tglobal_load_dwordx4 %1, %4, %5 offset:256 sc1\n\t"
                    "global_load_dwordx4 %2, %4, %5 offset:512 sc1\n\tglobal_load_dwordx4 %3, %4, %5 offset:768 sc1" END_IF
                    : "+v"(rsv[0]), "+v"(rsv[1]), "+v"(rsv[2]), "+v"(rsv[3]) : "v"(rso), "s"(ws), "s"(t) : "memory", "scc");
            });
            STAMP(2);
            asm volatile("s_waitcnt vmcnt(0)" : "+v"(rsv[0]), "+v"(rsv[1]), "+v"(rsv[2]), "+v"(rsv[3])::"memory");
#pragma unroll 1
            for (unsigned n = 0; n < SPIN_CAP; ++n) {
                bool ok = true;
#pragma unroll
                for (int mb = 0; mb < 4; ++mb) ok = ok && rsv[mb][1] == tag[0] && rsv[mb][3] == tag[0];
                if (__all(ok)) break;
                __builtin_amdgcn_s_sleep(2);
                asm volatile("" ::: "memory");
#pragma unroll
                for (int mb = 0; mb < 4; ++mb) rsv[mb] = ldw4(R, rso + mb * 256, 0);
            }
            f32x4 cs[4];
            read_cs(smem, lane, wid, cs);
            const int quarter = jj >> 2;
            const int sh = quarter == 0 ? 1 : quarter == 1 ? -1 : quarter == 2 ? 2 : -2;
#pragma unroll
            for (int mb = 0; mb < 4; ++mb) {
                const float mean = __uint_as_float(rsv[mb][0]), r = __uint_as_float(rsv[mb][2]);
                const float a = GC0 * r, bq = GC1 * r * r * r, k = __builtin_amdgcn_rcpf(r);
                const int t = rg * 64 + mb * 16 + c + sh, tw = t & (S_ - 1);
                const unsigned vo = (tw >> 4) * 32768 + (g * 16 + (tw & 15)) * 16;
#pragma unroll
                for (int p = 0; p < 2; ++p) {
                    u32x4 v = ln_gelu_chunk(acc[2 * p][mb], acc[2 * p + 1][mb], cs[2 * p], cs[2 * p + 1], mean, a, bq, k);
                    if (mb == 0 || mb == 3)
                        if (t != tw) v = (u32x4){0u, 0u, 0u, 0u};
                    stw4(R, vo, WS_HS + (2 * jj + (p ^ (wm >> 1))) * 1024, v);
                }
            }
            fp = lane == 0 || (lane == 1 && (rg == 0 || rg == 31));
            fo = WS_FH + jj * 8 + (lane == 0 ? (rg + 1) * 128 : rg == 0 ? 33 * 128 : 0);
        }
        DRAIN();
        if (fp) stw2(R, fo, 0, tag);
        if (wn) {
            float z[2] = {0.f, 0.f};
#pragma unroll
            for (int i = 0; i < 4; ++i)
#pragma unroll
                for (int e = 0; e < 4; ++e) {
                    const unsigned u = gx[i][e], w = gw[i & 1][e];
                    z[i >> 1] = dot2(cvtpk(bflo(u) * bflo(w), bfhi(u) * bfhi(w)), 0x3F803F80u, z[i >> 1]);
                }
            auto p1 = __builtin_amdgcn_permlane32_swap(__float_as_uint(z[0]), __float_as_uint(z[1]), false, false);
            float a1 = __uint_as_float(p1[0]) + __uint_as_float(p1[1]);
            a1 = dpp_add<0xB1>(a1); a1 = dpp_add<0x4E>(a1); a1 = dpp_add<0x141>(a1); a1 = dpp_add<0x140>(a1);
            a1 = rbf(sum16(a1));
            const float gt = rbf(1.0f / (1.0f + __expf(-a1)));
            if ((lane & 31) == 0) stw2(R, (srow + (lane >> 5)) * 8, WS_G, (u32x2){cvtpk(gt, 1.0f - gt), tag[0]});
        }
        STAMP(3);
    }
    const bool p3_on = jb < 28;
    const int mt3 = 4 * (xcd & 1) + (jb & 3), nt3 = 7 * (xcd >> 1) + (jb >> 2);
    LoadA L3;
    float mean3[4], rstd3[4], omg3[4];
    {
        int lane = fresh_lane();
        const int head = 4 * xcd + (jb >> 3), qt = jb & 7, qg = wid & 1, ks = wid >> 1, grp = wid >> 2;
        const int qblk0 = qt * 8 + qg * 4;
        {
            const int jh = head >> 1;
            const unsigned f = lane < 8 ? (16 + jh) * 32 + 8 * ks + lane : lane < 16 ? (32 + jh) * 32 + 8 * ks + lane - 8
                             : lane < 18 ? jh * 32 + 4 * qt + 2 * qg + lane - 16 : (16 + jh) * 32;
            wait_flags(R, WS_F1 + f * 8, tag);
        }
        STAMP(4);
        unsigned vl = lane * 16;
        const unsigned kvh = WS_KV + head * 64 * 4096;
        unsigned kvo = kvh + ks * 16 * 4096;
        bf16x8 Q[4][2], Ka[2], Kb[2], Va[2], Vb[2];
#define LDF(so, imm) BC8(ldw4(R, vl + (imm), so))
        Kb[0] = LDF(kvh, 0); Kb[1] = LDF(kvh, 1024);
#pragma unroll
        for (int i = 0; i < 4; ++i) {
            Q[i][0] = LDF(WS_Q + ((qblk0 + (i ^ ks)) * 32 + head) * 2048, 0);
            Q[i][1] = LDF(WS_Q + ((qblk0 + (i ^ ks)) * 32 + head) * 2048, 1024);
        }
        Ka[0] = LDF(kvo, 0); Ka[1] = LDF(kvo, 1024); Va[0] = LDF(kvo, 2048); Va[1] = LDF(kvo, 3072);
        float mref = -INFINITY;
#pragma unroll
        for (int i = 0; i < 4; ++i) {
            f32x16 s = MFMA32(Kb[0], Q[i][0], (f32x16){});
            s = MFMA32(Kb[1], Q[i][1], s);
#pragma unroll
            for (int r = 0; r < 16; r += 2) mref = __builtin_fmaxf(__builtin_fmaxf(s[r], s[r + 1]), mref);
        }
        mref = max32(mref);
        f32x16 O[4] = {};
        f32x4 Ls = {};
#define EX(X, i) X[i] = __builtin_amdgcn_exp2f(X[i])
#define CV(P, X, k) P[(k) >> 2][(k) & 3] = cvtpk(X[2 * (k)], X[2 * (k) + 1])
#define GRP4(Sc, Pc, j) EX(Sc, 4 * (j)); EX(Sc, 4 * (j) + 1); EX(Sc, 4 * (j) + 2); PIN(Sc); SBAR()
#define GRP4B(Sc, Pc, j) EX(Sc, 4 * (j) + 3); PIN(Sc); SBAR(); CV(Pc, Sc, 2 * (j)); PIN(Pc[(j) >> 1]); SBAR(); CV(Pc, Sc, 2 * (j) + 1); PIN(Pc[(j) >> 1]); SBAR()
#define STEP(Sc, Sn, Pc, Pp, Kn, qn, Vp, bp)                                                                    \
    do {                                                                                                        \
        SBAR();                                                                                                 \
        Sn = MFMA32(Kn[0], Q[qn][0], C); PIN(Sn); SBAR(); GRP4(Sc, Pc, 0); GRP4B(Sc, Pc, 0);                    \
        O[bp] = MFMA32(Vp[0], BC8(Pp[0]), O[bp]); PIN(O[bp]); SBAR(); GRP4(Sc, Pc, 1);                          \
        Ls = MFMA16(sel[bp], BC8(Pp[0]), Ls); PIN(Ls); SBAR(); GRP4B(Sc, Pc, 1);                                \
        Sn = MFMA32(Kn[1], Q[qn][1], Sn); PIN(Sn); SBAR(); GRP4(Sc, Pc, 2); GRP4B(Sc, Pc, 2);                   \
        O[bp] = MFMA32(Vp[1], BC8(Pp[1]), O[bp]); PIN(O[bp]); SBAR(); GRP4(Sc, Pc, 3);                          \
        Ls = MFMA16(sel[bp], BC8(Pp[1]), Ls); PIN(Ls); SBAR(); GRP4B(Sc, Pc, 3);                                \
    } while (0)
#define ATT_RUN(nit, TOP, MID, LASTV)                                                                              \
    do {                                                                                                        \
        Kb[0] = LDF(kvo + 4096, 0); Kb[1] = LDF(kvo + 4096, 1024);                                              \
        bf16x8 sel[4];                                                                                          \
        _Pragma("unroll") for (int i = 0; i < 4; ++i) {                                                         \
            const short v = ((lane & 3) == i && ((lane >> 2) & 1) == ((lane >> 4) & 1)) ? (short)0x3F80 : (short)0; \
            sel[i] = (bf16x8){v, v, v, v, v, v, v, v};                                                          \
            PIN(sel[i]);                                                                                        \
        }                                                                                                       \
        f32x16 C, SA, SB;                                                                                       \
        _Pragma("unroll") for (int r = 0; r < 16; ++r) C[r] = -mref;                                            \
        PIN(C);                                                                                                 \
        u32x4 PA[2] = {}, PB[2] = {};                                                                           \
        Vb[0] = Vb[1] = (bf16x8){};                                                                             \
        SA = MFMA32(Ka[0], Q[0][0], C);                                                                         \
        SA = MFMA32(Ka[1], Q[0][1], SA);                                                                        \
        _Pragma("unroll 1") for (int it = 0; it < (nit); ++it) {                                                \
            const bool last = it == (nit) - 1;                                                                  \
            const unsigned n2 = last ? kvo : kvo + 8192, nv = last ? (LASTV) : n2;                              \
            TOP;                                                                                                \
            STEP(SA, SB, PA, PB, Ka, 1, Vb, 3);                                                                 \
            Vb[0] = LDF(kvo + 4096, 2048); Vb[1] = LDF(kvo + 4096, 3072);                                       \
            STEP(SB, SA, PB, PA, Ka, 2, Va, 0);                                                                 \
            MID;                                                                                                \
            STEP(SA, SB, PA, PB, Ka, 3, Va, 1);                                                                 \
            Ka[0] = LDF(n2, 0); Ka[1] = LDF(n2, 1024);                                                          \
            STEP(SB, SA, PB, PA, Kb, 0, Va, 2);                                                                 \
            MID;                                                                                                \
            STEP(SA, SB, PA, PB, Kb, 1, Va, 3);                                                                 \
            Va[0] = LDF(nv, 2048); Va[1] = LDF(nv, 3072);                                                       \
            STEP(SB, SA, PB, PA, Kb, 2, Vb, 0);                                                                 \
            MID;                                                                                                \
            STEP(SA, SB, PA, PB, Kb, 3, Vb, 1);                                                                 \
            Kb[0] = LDF(n2 + 4096, 0); Kb[1] = LDF(n2 + 4096, 1024);                                            \
            STEP(SB, SA, PB, PA, Ka, 0, Vb, 2);                                                                 \
            MID;                                                                                                \
            kvo += 8192;                                                                                        \
        }                                                                                                       \
        O[3] = MFMA32(Vb[0], BC8(PB[0]), O[3]);                                                                 \
        O[3] = MFMA32(Vb[1], BC8(PB[1]), O[3]);                                                                 \
        Ls = MFMA16(sel[3], BC8(PB[0]), Ls);                                                                    \
        Ls = MFMA16(sel[3], BC8(PB[1]), Ls);                                                                    \
    } while (0)
        const int mt2 = 4 * (xcd >> 1) + (jb & 3), nt2 = 8 * (xcd & 1) + (jb >> 2);
        const int n1 = grp ? 5 : 3;
        {
            const int ir = lane >> 3;
            const unsigned wv = ir * 2048 + (((lane & 7) ^ (((ir >> 1) & 3) << 1)) << 4);
            const char* wp = (const char*)(w2 + (size_t)(128 * wid) * D_ + nt2 * 64);
            unsigned wd = lds0 + wid * 16384;
            STAMP(5);
            ATT_RUN(n1, if (it == 3) BAR(),
                    if (it < 2) {
                        GLDS(wd, wv, wp, 0, ""); wd += 1024; wp += 16384;
                        GLDS(wd, wv, wp, 0, ""); wd += 1024; wp += 16384;
                    },
                    kvo);
            if (!grp) BAR();
            STAMP(6);
        }
        {
            lane = fresh_lane();
            vl = lane * 16;
            const int c = lane & 15, g = lane >> 4, q = c >> 2, mb = mt2 * 8 + wid;
            wait_flags(R, WS_FH + (((mb * 16 - 2) >> 6) + 1) * 128 + (lane & 31) * 8, tag);
            const unsigned hso = WS_HS + mb * 32768;
            bf16x8 X[16];
#pragma unroll
            for (int i = 0; i < 16; ++i) X[i] = LDF(hso + (i >> 2) * 4096, (i & 3) * 1024);
            u32x4 xr[2];
#pragma unroll
            for (int p = 0; p < 2; ++p)
                xr[p] = *(const u32x4*)(x + (size_t)(mb * 16 + c) * D_ + nt2 * 64 + p * 32 + (g & 1) * 16 + (g >> 1) * 8);
            lds_ptr wb[4];
#pragma unroll
            for (int nb = 0; nb < 4; ++nb)
                wb[nb] = (lds_ptr)smem + (4 * g + q) * 128 + ((nb ^ ((2 * g + (q >> 1)) & 3)) << 5) + (lane & 3) * 8;
            f32x4 a2[4] = {};
            bf16x8 W[2][4];
#pragma unroll
            for (int nb = 0; nb < 4; ++nb) W[0][nb] = ldtr(wb[nb]);
#pragma unroll
            for (int kb = 0; kb < 32; ++kb) {
                if (kb == 15)
#pragma unroll
                    for (int nb = 0; nb < 4; ++nb) { wb[nb] += 65536; PIN(wb[nb]); }
                if (kb < 31)
#pragma unroll
                    for (int nb = 0; nb < 4; ++nb) W[(kb + 1) & 1][nb] = ldtr(wb[nb] + ((kb + 1) & 15) * 4096);
                SBAR();
#pragma unroll
                for (int nb = 0; nb < 4; ++nb) a2[nb] = MFMA16(W[kb & 1][nb], X[kb & 15], a2[nb]);
                SBAR();
                if (kb < 16) X[kb] = LDF(hso + ((kb + 16) >> 2) * 4096, (kb & 3) * 1024);
                if (kb == 20) { Ka[0] = LDF(kvo, 0); Ka[1] = LDF(kvo, 1024); Va[0] = LDF(kvo, 2048); Va[1] = LDF(kvo, 3072); }
            }
            float s1 = 0.f, s2 = 0.f;
#pragma unroll
            for (int p = 0; p < 2; ++p) {
                auto u = __builtin_amdgcn_permlane16_swap(xr[p][0], xr[p][2], false, false);
                auto v = __builtin_amdgcn_permlane16_swap(xr[p][1], xr[p][3], false, false);
                const f32x4 lo = {bflo(u[0]), bfhi(u[0]), bflo(v[0]), bfhi(v[0])}, hi = {bflo(u[1]), bfhi(u[1]), bflo(v[1]), bfhi(v[1])};
                const u32x4 o = pack44(a2[2 * p] + lo, a2[2 * p + 1] + hi);
#pragma unroll
                for (int e = 0; e < 4; ++e) { const unsigned w = o[e]; s1 = dot2(w, 0x3F803F80u, s1); s2 = dot2(w, w, s2); }
                stw4(R, vl, WS_X1 + (mb * 32 + nt2 * 2 + p) * 1024, o);
            }
            s1 = sum32(sum16(s1)); s2 = sum32(sum16(s2));
            if (g == 0) stw2(R, c * 8, WS_ST + (mb * 16 + nt2) * 128, (u32x2){__float_as_uint(s1), __float_as_uint(s2)});
            post_flag(R, WS_F2 + (mt2 >> 1) * 2048 + (((mt2 & 1) * 16 + nt2) * 8 + wid) * 8, tag, lane);
            STAMP(7);
        }
        setupA<true>(L3, lane, wid, lds0, ws + WS_X1 + (size_t)mt3 * 16 * 32768, (const char*)(w3 + nt3 * 128), F_ * 2,
                     (const char*)(w3 + nt3 * 128 + 64), F_ * 2);
        const unsigned f2o = WS_F2 + mt3 * 2048;
        ATT_RUN(8 - n1, if (last) BAR(), , f2o - 2048);
        STAMP(8);
#undef ATT_RUN
#undef STEP
#undef GRP4
#undef GRP4B
        const int blk = qblk0 + ks;
        u32x2 gv = ldw2(R, (blk * 32 + (lane & 31)) * 8, WS_G);
        u32x2 stp[4][4], og[4];
        if (p3_on) {
            A_W0(L3, 0); A_W1(L3, 0); L3.w0 += L3.wstep0; L3.w1 += L3.wstep1; A_W0(L3, STG_A); L3.w0 += L3.wstep0;
            const u32x4 f0 = __builtin_bit_cast(u32x4, Va[0]), f1 = __builtin_bit_cast(u32x4, Va[1]);
            if (!__all(f0[0] == tag[0] && f0[1] == tag[1] && f0[2] == tag[0] && f0[3] == tag[1] && f1[0] == tag[0] &&
                       f1[1] == tag[1] && f1[2] == tag[0] && f1[3] == tag[1])) {
#pragma unroll 1
                for (int i = 0; i < 4; ++i) wait_flags(R, f2o + i * 512 + lane * 8, tag);
            }
            A_X(L3, 0, 0, true); A_X(L3, 1, 0, true); A_X(L3, 2, 0, true); A_X(L3, 3, 0, true);
            L3.xs += L3.xstep;
            A_X(L3, 0, STG_A, true); A_X(L3, 1, STG_A, true); A_X(L3, 2, STG_A, true); A_X(L3, 3, STG_A, true);
            L3.xs += L3.xstep;
#pragma unroll
            for (int mb = 0; mb < 4; ++mb) {
                const int mblk = mt3 * 16 + (wid & 3) * 4 + mb;
#pragma unroll
                for (int i = 0; i < 4; ++i) stp[mb][i] = ldw2(R, (lane >> 4) * 512 + (lane & 15) * 8 + i * 128, WS_ST + mblk * 2048);
                og[mb] = ldw2(R, (lane & 15) * 8, WS_G + mblk * 128);
            }
        }
        constexpr unsigned CMB = 88 * 1024;
        f32x16 o;
        float l;
        {
            LDS_AS f32x4* me = (LDS_AS f32x4*)((lds_ptr)smem + CMB + wid * 8192) + lane;
            LDS_AS f32x2* ml = (LDS_AS f32x2*)((lds_ptr)smem + CMB + 65536 + wid * 512) + lane;
#pragma unroll
            for (int i = 0; i < 2; ++i)
#pragma unroll
                for (int e = 0; e < 4; ++e)
                    me[(i * 4 + e) * 64] = (f32x4){O[2 + i][4 * e], O[2 + i][4 * e + 1], O[2 + i][4 * e + 2], O[2 + i][4 * e + 3]};
            *ml = (f32x2){Ls[2], Ls[3]};
            LG_BAR();
            LDS_AS f32x4* pa = (LDS_AS f32x4*)((lds_ptr)smem + CMB + (wid ^ 4) * 8192) + lane;
#pragma unroll
            for (int i = 0; i < 2; ++i)
#pragma unroll
                for (int e = 0; e < 4; ++e) {
                    const f32x4 v = pa[(i * 4 + e) * 64];
#pragma unroll
                    for (int j = 0; j < 4; ++j) O[i][4 * e + j] += v[j];
                }
            const f32x2 l2 = *((const LDS_AS f32x2*)((lds_ptr)smem + CMB + 65536 + (wid ^ 4) * 512) + lane) + (f32x2){Ls[0], Ls[1]};
#pragma unroll
            for (int e = 0; e < 4; ++e) pa[e * 64] = (f32x4){O[1][4 * e], O[1][4 * e + 1], O[1][4 * e + 2], O[1][4 * e + 3]};
            ((LDS_AS float*)(pa + 256 - lane))[lane] = l2[1];
            LG_BAR();
            const LDS_AS f32x4* p2 = (const LDS_AS f32x4*)((lds_ptr)smem + CMB + (wid ^ 6) * 8192) + lane;
#pragma unroll
            for (int e = 0; e < 4; ++e) {
                const f32x4 v = p2[e * 64];
#pragma unroll
                for (int j = 0; j < 4; ++j) o[4 * e + j] = O[0][4 * e + j] + v[j];
            }
            l = ((const LDS_AS float*)(p2 + 256 - lane))[lane] + l2[0];
            LG_BAR();
        }
        if (p3_on) { A_W1(L3, STG_A); L3.w1 += L3.wstep1; }
        {
            float n = 0.f;
#pragma unroll
            for (int r = 0; r < 16; ++r) n = __builtin_fmaf(o[r], 0.f, n);
            const bool ok = l >= 0x1p-50f && l <= 0x1p50f && n == 0.f;
            if (__builtin_expect(!__all(ok), 0)) {
                const bf16x8 q0 = LDF(WS_Q + (blk * 32 + head) * 2048, 0), q1 = LDF(WS_Q + (blk * 32 + head) * 2048, 1024);
                float m = -INFINITY;
#pragma unroll 1
                for (int t = 0; t < 64; ++t) {
                    f32x16 s = MFMA32(LDF(kvh + t * 4096, 0), q0, (f32x16){});
                    s = MFMA32(LDF(kvh + t * 4096, 1024), q1, s);
#pragma unroll
                    for (int r = 0; r < 16; ++r) m = __builtin_fmaxf(m, s[r]);
                }
                m = max32(m);
                o = (f32x16){};
                l = 0.f;
#pragma unroll 1
                for (int t = 0; t < 64; ++t) {
                    f32x16 s = MFMA32(LDF(kvh + t * 4096, 0), q0, (f32x16){});
                    s = MFMA32(LDF(kvh + t * 4096, 1024), q1, s);
                    u32x4 p[2];
#pragma unroll
                    for (int r = 0; r < 8; ++r) {
                        const unsigned w = cvtpk(__builtin_amdgcn_exp2f(s[2 * r] - m), __builtin_amdgcn_exp2f(s[2 * r + 1] - m));
                        p[r >> 2][r & 3] = w;
                        l += bflo(w) + bfhi(w);
                    }
                    o = MFMA32(LDF(kvh + t * 4096, 2048), BC8(p[0]), o);
                    o = MFMA32(LDF(kvh + t * 4096, 3072), BC8(p[1]), o);
                }
                l = sum32(l);
            }
        }
#pragma unroll 1
        for (unsigned n = 0; n < SPIN_CAP; ++n) {
            if (__all(gv[1] == tag[0])) break;
            __builtin_amdgcn_s_sleep(2);
            asm volatile("" ::: "memory");
            gv = ldw2(R, (blk * 32 + (lane & 31)) * 8, WS_G);
        }
        {
            const float sc = bflo(gv[0]) / l;
            const unsigned vo = ((lane >> 4) & 1) * (144 * 1024) + ((lane >> 5) * 16 + (lane & 15)) * 16;
#pragma unroll
            for (int u = 0; u < 2; ++u)
                stw4(R, vo + u * 512, WS_A4 + (blk * 2 * 144 + (head >> 4) * 72 + (head & 15)) * 1024,
                     pack44((f32x4){o[4 * u], o[4 * u + 1], o[4 * u + 2], o[4 * u + 3]} * sc,
                            (f32x4){o[8 + 4 * u], o[9 + 4 * u], o[10 + 4 * u], o[11 + 4 * u]} * sc));
        }
        if (p3_on) {
#pragma unroll 1
            for (unsigned n = 0; n < SPIN_CAP; ++n) {
                if (__all(og[0][1] == tag[0] && og[1][1] == tag[0] && og[2][1] == tag[0] && og[3][1] == tag[0])) break;
                __builtin_amdgcn_s_sleep(2);
                asm volatile("" ::: "memory");
#pragma unroll
                for (int mb = 0; mb < 4; ++mb) og[mb] = ldw2(R, (lane & 15) * 8, WS_G + (mt3 * 16 + (wid & 3) * 4 + mb) * 128);
            }
#pragma unroll
            for (int mb = 0; mb < 4; ++mb) {
                float s1 = 0.f, s2 = 0.f;
#pragma unroll
                for (int i = 0; i < 4; ++i) { s1 += __uint_as_float(stp[mb][i][0]); s2 += __uint_as_float(stp[mb][i][1]); }
                s1 = sum32(sum16(s1)); s2 = sum32(sum16(s2));
                mean3[mb] = s1 * (1.0f / D_);
                rstd3[mb] = __builtin_amdgcn_rsqf(__builtin_fmaxf(s2 * (1.0f / D_) - mean3[mb] * mean3[mb], 0.f) + 1e-5f);
                omg3[mb] = bfhi(og[mb][0]);
            }
        }
        post_flag(R, WS_FO + (((blk >> 2) * 2 + (head >> 4)) * 64 + (head & 15) * 4 + (blk & 3)) * 8, tag, lane);
        STAMP(9);
#undef LDF
#undef EX
#undef CV
    }
    {
        const int lane = fresh_lane(), c = lane & 15, g = lane >> 4, q = c >> 2;
        const int ks = xcd & 1, mt4 = 8 * ((xcd >> 1) & 1) + (jb & 7), nt4 = 4 * (xcd >> 2) + (jb >> 3);
        const int j = wid & 3, grp = wid >> 2, wm = j & 1, wn = j >> 1;
        constexpr unsigned STG_B = 32768;
        const unsigned xv0 = (2 * (j ^ grp ^ 1) + ks) * (144 * 1024) + lane * 16 + 2048;
        const unsigned xv1 = (2 * (j ^ grp ^ 1) + (ks ^ 1)) * (144 * 1024) + lane * 16;
        const int ir = 16 * j + (lane >> 3);
        const unsigned wva = ir * 2048 + (((lane & 7) ^ (((ir >> 1) & 3) << 1)) << 4), wvb = wva + 8 * 2048 - 1024;
        const char* xs4 = ws + WS_A4 + ((size_t)mt4 * 8 * 144 + ks * 72) * 1024 - 2048;
        const char* wo4 = (const char*)(wo + (size_t)ks * 512 * D_ + nt4 * 128);
        const char* w44 = (const char*)(w4 + (size_t)ks * 1792 * D_ + nt4 * 128) - 8 * 131072;
        const unsigned dx4 = lds0 + j * 4096, dw4 = lds0 + 16384 + j * 2048;
#define B_SET(tile)                                                                          \
    const int tl_ = (tile);                                                                  \
    const char* bx_ = xs4 + tl_ * 2048;                                                      \
    const char* bw_ = (tl_ < 8 ? wo4 : w44) + tl_ * 131072;                                  \
    const char* bw1_ = bw_ + 128;                                                            \
    const unsigned bo_ = (tl_ & 3) * STG_B
#define B_X0() GLDS(dx4 + bo_, xv0, bx_, 0, " sc1")
#define B_X1() GLDS(dx4 + bo_, xv0, bx_, 1024, " sc1")
#define B_X2() GLDS(dx4 + bo_, xv1, bx_, 2048, " sc1")
#define B_X3() GLDS(dx4 + bo_, xv1, bx_, 3072, " sc1")
#define B_W0() GLDS(dw4 + bo_, wva, bw_, 0, "")
#define B_W1() GLDS(dw4 + bo_, wvb, bw_, 1024, "")
#define B_W2() GLDS(dw4 + 8192 + bo_, wva, bw1_, 0, "")
#define B_W3() GLDS(dw4 + 8192 + bo_, wvb, bw1_, 1024, "")
#define B_ALL(tile) do { B_SET(tile); B_W0(); B_W1(); B_W2(); B_W3(); B_X0(); B_X1(); B_X2(); B_X3(); } while (0)
        const unsigned foo = WS_FO + (mt4 * 2 + ks) * 512 + lane * 8;
        if (p3_on) {
            BAR();
            if (grp) BAR();
            STAMP(10);
            f32x4 acc[4][4] = {};
            u32x2 fo = {0u, 0u};
            kloopA<true, 2>(smem, L3, lane, wid, acc, [&](int t) {
                asm volatile(IF_EQ("%3", 14) "global_load_dwordx2 %0, %1, %2 sc1" END_IF
                             : "+v"(fo) : "v"(foo), "s"(ws), "s"(t) : "memory", "scc");
            });
            STAMP(11);
            if (grp) LG_BAR();
            asm volatile("s_waitcnt vmcnt(0)" : "+v"(fo)::"memory");
            if (!__all(fo[0] == tag[0] && fo[1] == tag[1])) wait_flags(R, foo, tag);
            if (grp) { B_ALL(0); B_ALL(2); } else { B_ALL(1); }
            f32x4 cs[4];
            read_cs(smem, lane, wid, cs);
            const unsigned a4o = WS_A4 + ((mt3 * 16 + j * 4) * 144 + (nt3 / 14) * 72 + 16 + (nt3 % 14) * 4 + grp * 2) * 1024;
#pragma unroll
            for (int mb = 0; mb < 4; ++mb) {
                const float r = rstd3[mb], a = GC0 * r, bq = GC1 * r * r * r;
                const float k = __builtin_fminf(__builtin_amdgcn_rcpf(r * omg3[mb]), 1e30f);
#pragma unroll
                for (int p = 0; p < 2; ++p)
                    stw4(R, lane * 16, a4o + (mb * 144 + (p ^ (j >> 1))) * 1024,
                         ln_gelu_chunk(acc[2 * p][mb], acc[2 * p + 1][mb], cs[2 * p], cs[2 * p + 1], mean3[mb], a, bq, k));
                if (mb == 0) { SBAR(); if (!grp) BAR(); SBAR(); }
            }
            post_flag(R, WS_F3 + (((mt3 * 2 + (j >> 1)) * 2 + nt3 / 14) * 64 + (nt3 % 14) * 4 + grp * 2 + (j & 1)) * 8, tag, lane);
        } else {
            wait_flags(R, foo, tag);
            if (grp) { B_ALL(0); B_ALL(2); } else { B_ALL(1); }
            DRAIN();
        }
        STAMP(12);
        BAR();
        if (grp) BAR();
        STAMP(13);
        lds_ptr xb = (lds_ptr)smem + grp * STG_B + (8 * wm) * 1024 + lane * 16, wb[4];
#pragma unroll
        for (int nb = 0; nb < 4; ++nb)
            wb[nb] = (lds_ptr)smem + grp * STG_B + 16384 + wn * 8192 + (4 * g + q) * 128 + ((nb ^ ((2 * g + (q >> 1)) & 3)) << 5) + (lane & 3) * 8;
        f32x4 acc[4][4] = {};
        const unsigned f3o = WS_F3 + (mt4 * 2 + ks) * 512 + (lane < 56 ? lane : 0) * 8;
        const int mblk = mt4 * 8 + wm * 4 + grp * 2 + ks;
        u32x2 og = {0u, 0u};
        const unsigned xo = WS_X1 + (mblk * 32 + nt4 * 4 + wn * 2) * 1024 + lane * 16, go = WS_G + mblk * 128 + c * 8;
        u32x4 xr[2] = {};
#pragma unroll 1
        for (int t = grp; t < 36; t += 2) {
            bf16x8 xf[4][2], wf[4][2];
            const bool more = t < 33;
            B_SET(t + 3);
            SBAR();
            u32x2 f3;
            asm volatile(IF_EQ("%3", 4) "global_load_dwordx2 %0, %1, %2 sc1" END_IF
                         : "=v"(f3) : "v"(f3o), "s"(ws), "s"(t) : "memory", "scc");
            if (more) { B_W0(); B_X0(); }
            SBAR();
            wf[0][0] = ldtr(wb[0]); xf[0][0] = ld128(xb); wf[1][0] = ldtr(wb[1]); xf[1][0] = ld128(xb + 2048);
            SBAR();
            if (more) { B_W1(); B_X1(); }
            SBAR();
            wf[2][0] = ldtr(wb[2]); xf[2][0] = ld128(xb + 4096); wf[3][0] = ldtr(wb[3]); xf[3][0] = ld128(xb + 6144);
            SBAR();
            if (more) { B_W2(); B_X2(); }
            SBAR();
            wf[0][1] = ldtr(wb[0] + 4096); xf[0][1] = ld128(xb + 1024); wf[1][1] = ldtr(wb[1] + 4096); xf[1][1] = ld128(xb + 3072);
            wf[2][1] = ldtr(wb[2] + 4096); xf[2][1] = ld128(xb + 5120); wf[3][1] = ldtr(wb[3] + 4096); xf[3][1] = ld128(xb + 7168);
            SBAR();
            if (more) {
                B_W3(); B_X3();
                asm volatile("s_waitcnt vmcnt(8)" : "+v"(f3)::"memory");
                if (t == 4)
                    if (!__all(f3[0] == tag[0] && f3[1] == tag[1])) wait_flags(R, f3o, tag);
                LG_BAR();
            } else if (t < 35) {
                WAIT_BAR(0);
            } else {
                LG_BAR();
            }
            asm volatile(IF_EQ("%6", 17)
                "global_load_dwordx4 %0, %3, %5 sc1\n\tglobal_load_dwordx4 %1, %3, %5 offset:1024 sc1\n\t"
                "global_load_dwordx2 %2, %4, %5 sc1" END_IF
                : "+v"(xr[0]), "+v"(xr[1]), "+v"(og) : "v"(xo), "v"(go), "s"(ws), "s"((t + 1) >> 1) : "memory", "scc");
            SBAR();
            const int adv = (t & 2) ? -2 * (int)STG_B : 2 * (int)STG_B;
            __builtin_amdgcn_s_setprio(1);
#pragma unroll
            for (int i = 0; i < 32; ++i) {
                acc[i & 3][(i >> 2) & 3] = MFMA16(wf[i & 3][i >> 4], xf[(i >> 2) & 3][i >> 4], acc[i & 3][(i >> 2) & 3]);
                if (i == 3) { xb += adv; PIN(xb); }
                if (i >= 4 && i < 8) { wb[i & 3] += adv; PIN(wb[i & 3]); }
                SBAR();
            }
            __builtin_amdgcn_s_setprio(0);
            SBAR();
            if (t != 35) BAR();
        }
        STAMP(14);
        f32x4 res[4][2];
        {
            LDS_AS f32x4* ex = (LDS_AS f32x4*)((lds_ptr)smem + wid * 8192) + lane;
            const LDS_AS f32x4* ey = (const LDS_AS f32x4*)((lds_ptr)smem + (wid ^ 4) * 8192) + lane;
#pragma unroll
            for (int i = 0; i < 8; ++i) ex[i * 64] = acc[i & 3][2 + (i >> 2)];
            LG_BAR();
#pragma unroll
            for (int i = 0; i < 8; ++i) res[i & 3][i >> 2] = ey[i * 64] + acc[i & 3][i >> 2];
        }
        const int me = (mt4 * 8 + nt4) * 2 + ks;
#pragma unroll
        for (int nb = 0; nb < 4; ++nb)
            stw4(R, lane * 16 + nb * 1024, WS_EX + me * 32768 + wid * 4096, __builtin_bit_cast(u32x4, res[nb][1]));
        asm volatile("s_waitcnt vmcnt(0)" : "+v"(xr[0]), "+v"(xr[1]), "+v"(og)::"memory");
        if (lane == 0) stw2(R, 0, WS_FX + (me * 8 + wid) * 8, tag);
        STAMP(15);
        wait_flags(R, WS_FX + ((me ^ 1) * 8 + wid) * 8, tag);
        STAMP(16);
        u32x4 pv[4];
#pragma unroll
        for (int nb = 0; nb < 4; ++nb) pv[nb] = ldw4(R, lane * 16 + nb * 1024, WS_EX + (me ^ 1) * 32768 + wid * 4096);
#pragma unroll 1
        for (unsigned n = 0; n < SPIN_CAP; ++n) {
            if (__all(og[1] == tag[0])) break;
            __builtin_amdgcn_s_sleep(2);
            asm volatile("" ::: "memory");
            og = ldw2(R, go, 0);
        }
        const float omg = bfhi(og[0]);
#pragma unroll
        for (int p = 0; p < 2; ++p) {
            const f32x4 lo = {bflo(xr[p][0]), bfhi(xr[p][0]), bflo(xr[p][1]), bfhi(xr[p][1])};
            const f32x4 hi = {bflo(xr[p][2]), bfhi(xr[p][2]), bflo(xr[p][3]), bfhi(xr[p][3])};
            const u32x4 o = pack44(lo * omg + (res[2 * p][0] + __builtin_bit_cast(f32x4, pv[2 * p])),
                                   hi * omg + (res[2 * p + 1][0] + __builtin_bit_cast(f32x4, pv[2 * p + 1])));
            auto u = __builtin_amdgcn_permlane16_swap(o[0], o[2], false, false);
            auto v = __builtin_amdgcn_permlane16_swap(o[1], o[3], false, false);
            *(u32x4*)(out + (size_t)(mblk * 16 + c) * D_ + nt4 * 128 + wn * 64 + p * 32 + (g & 1) * 16 + (g >> 1) * 8) =
                (u32x4){u[0], v[0], u[1], v[1]};
        }
        if (b == 0 && threadIdx.x == 0) stw2(R, 0, WS_EPOCH, tag);
        STAMP(17);
    }
}

extern "C" void kernel_launch(void* const* d_in, const int* in_sizes, int n_in, void* d_out, int out_size, void* d_ws,
                              size_t ws_size, hipStream_t stream) {
    (void)hipFuncSetAttribute((const void*)k_mega, hipFuncAttributeMaxDynamicSharedMemorySize, LDS_BYTES);
    k_mega<<<256, 512, LDS_BYTES, stream>>>((const u16*)d_in[0], (const u16*)d_in[1], (const u16*)d_in[2], (const u16*)d_in[4],
                                             (const u16*)d_in[5], (const u16*)d_in[6], (const u16*)d_in[7], (const u16*)d_in[8],
                                             (char*)d_ws, (u16*)d_out);
}
```

```cpp
#include <hip/hip_runtime.h>
#include <hip/hip_bf16.h>
#include <stdint.h>

typedef unsigned short u16;
typedef unsigned long long u64;
typedef short bf16x8 __attribute__((ext_vector_type(8)));
typedef short v4i16_t __attribute__((ext_vector_type(4)));
typedef __bf16 bf2_t __attribute__((ext_vector_type(2)));
typedef float f32x16 __attribute__((ext_vector_type(16)));
typedef float f32x4 __attribute__((ext_vector_type(4)));
typedef float f32x2 __attribute__((ext_vector_type(2)));
typedef unsigned u32x4 __attribute__((ext_vector_type(4)));
typedef unsigned u32x2 __attribute__((ext_vector_type(2)));
typedef __attribute__((address_space(3))) char* lds_ptr;
typedef __amdgpu_buffer_rsrc_t rsrc_t;
#define DEV __device__ __forceinline__
#define LDS_AS __attribute__((address_space(3)))
#define SBAR() __builtin_amdgcn_sched_barrier(0)
#define PIN(x) asm volatile("" : "+v"(x))
#define MFMA32(a, b, c) __builtin_amdgcn_mfma_f32_32x32x16_bf16(a, b, c, 0, 0, 0)
#define MFMA16(a, b, c) __builtin_amdgcn_mfma_f32_16x16x32_bf16(a, b, c, 0, 0, 0)
#define BAR() asm volatile("s_barrier" ::: "memory")
#define LGKM0() __builtin_amdgcn_s_waitcnt(0xC07F)
#define WAIT_BAR(N) do { LGKM0(); asm volatile("s_waitcnt vmcnt(" #N ")\n\ts_barrier" ::: "memory"); } while (0)
#define LG_BAR() do { asm volatile("" ::: "memory"); LGKM0(); asm volatile("s_barrier" ::: "memory"); } while (0)
#define DRAIN() asm volatile("s_waitcnt vmcnt(0)" ::: "memory")
#define BC8(x) __builtin_bit_cast(bf16x8, x)

template <int N> struct IC { static constexpr int value = N; };
constexpr int S_ = 2048, D_ = 1024, F_ = 3584;
constexpr unsigned KB = 1024, MB = 1u << 20;
constexpr unsigned WS_EPOCH = 0;
constexpr unsigned WS_F1 = 4 * KB;
constexpr unsigned WS_FH = 12 * KB;
constexpr unsigned WS_F2 = 24 * KB;
constexpr unsigned WS_FO = 40 * KB;
constexpr unsigned WS_F3 = 56 * KB;
constexpr unsigned WS_FX = 72 * KB;
constexpr unsigned WS_RS = 128 * KB;
constexpr unsigned WS_G = 160 * KB;
constexpr unsigned WS_ST = 256 * KB;
constexpr unsigned WS_KV = 5 * MB;
constexpr unsigned WS_HS = 13 * MB;
constexpr unsigned WS_X1 = 17 * MB;
constexpr unsigned WS_A4 = 21 * MB;
constexpr unsigned WS_EX = 40 * MB;
constexpr int LDS_BYTES = 160 * 1024;
constexpr unsigned SPIN_CAP = 1u << 18;

#define STAMP(k)
#define STAMP_INIT()
#define STAMP_OUT()

DEV unsigned cvtpk(float lo, float hi) {
    const f32x2 v = {lo, hi};
    return __builtin_bit_cast(unsigned, __builtin_convertvector(v, bf2_t));
}
DEV float bflo(unsigned u) { return __uint_as_float(u << 16); }
DEV float bfhi(unsigned u) { return __uint_as_float(u & 0xffff0000u); }
DEV float rbf(float f) { return bflo(cvtpk(f, f)); }
DEV float dot2(unsigned a, unsigned b, float c) {
    return __builtin_amdgcn_fdot2_f32_bf16(__builtin_bit_cast(bf2_t, a), __builtin_bit_cast(bf2_t, b), c, false);
}
DEV u32x4 pack44(const f32x4& a, const f32x4& b) {
    return (u32x4){cvtpk(a[0], a[1]), cvtpk(a[2], a[3]), cvtpk(b[0], b[1]), cvtpk(b[2], b[3])};
}
DEV float sum32(float v) {
    auto r = __builtin_amdgcn_permlane32_swap(__float_as_uint(v), __float_as_uint(v), false, false);
    return __uint_as_float(r[0]) + __uint_as_float(r[1]);
}
DEV float sum16(float v) {
    auto r = __builtin_amdgcn_permlane16_swap(__float_as_uint(v), __float_as_uint(v), false, false);
    return __uint_as_float(r[0]) + __uint_as_float(r[1]);
}
DEV float max32(float v) {
    auto r = __builtin_amdgcn_permlane32_swap(__float_as_uint(v), __float_as_uint(v), false, false);
    return __builtin_fmaxf(__uint_as_float(r[0]), __uint_as_float(r[1]));
}
template <int CTRL>
DEV float dpp_add(float v) {
    return v + __uint_as_float(__builtin_amdgcn_update_dpp(0u, __float_as_uint(v), CTRL, 0xf, 0xf, true));
}
DEV float wave_sum(float v) {
    v = dpp_add<0xB1>(v);
    v = dpp_add<0x4E>(v);
    v = dpp_add<0x141>(v);
    v = dpp_add<0x140>(v);
    return sum32(sum16(v));
}
DEV bf16x8 ld128(lds_ptr p) { return *(const LDS_AS bf16x8*)p; }
template <int H2 = 2048>
DEV bf16x8 ldtr(lds_ptr p) {
    v4i16_t a = __builtin_amdgcn_ds_read_tr16_b64_v4i16((LDS_AS v4i16_t*)p);
    v4i16_t b = __builtin_amdgcn_ds_read_tr16_b64_v4i16((LDS_AS v4i16_t*)(p + H2));
    return (bf16x8){a[0], a[1], a[2], a[3], b[0], b[1], b[2], b[3]};
}
DEV int rfl(int v) { return __builtin_amdgcn_readfirstlane(v); }
DEV int fresh_lane() {
    int l = __builtin_amdgcn_mbcnt_hi(~0u, __builtin_amdgcn_mbcnt_lo(~0u, 0u));
    PIN(l);
    return l;
}
DEV u32x4 ldw4(rsrc_t r, unsigned v, unsigned s) { return __builtin_amdgcn_raw_buffer_load_b128(r, v, s, 16); }
DEV u32x2 ldw2(rsrc_t r, unsigned v, unsigned s) { return __builtin_amdgcn_raw_buffer_load_b64(r, v, s, 16); }
DEV void stw4(rsrc_t r, unsigned v, unsigned s, u32x4 x) {
    __builtin_amdgcn_raw_buffer_store_b128(x, r, v, s, 16);
    asm volatile("s_nop 1" ::"v"(x) : "memory");
}
DEV void stw2(rsrc_t r, unsigned v, unsigned s, u32x2 x) { __builtin_amdgcn_raw_buffer_store_b64(x, r, v, s, 16); }
#define GLDS(dst, voff, sptr, IMM, SC)                                                                        \
    asm volatile("s_mov_b32 m0, %0\n\ts_nop 0\n\tglobal_load_lds_dwordx4 %1, %2 offset:" #IMM SC ::"s"(dst), "v"(voff), \
                 "s"(sptr) : "memory")
#define GLDS1(dst, voff, sptr) asm volatile("s_mov_b32 m0, %0\n\ts_nop 0\n\tglobal_load_lds_dword %1, %2" ::"s"(dst), "v"(voff), "s"(sptr) : "memory")
#define IF_EQ(t, K) "s_cmp_lg_u32 " t ", " #K "\n\ts_cbranch_scc1 .Lskip%=\n\t"
#define END_IF "\n.Lskip%=:"
DEV void post_flag(rsrc_t r, unsigned off, u32x2 tag, int lane) {
    DRAIN();
    if (lane == 0) stw2(r, 0, off, tag);
}
DEV void wait_flags(rsrc_t r, unsigned voff, u32x2 tag) {
#pragma unroll 1
    for (unsigned n = 0; n < SPIN_CAP; ++n) {
        asm volatile("" ::: "memory");
        const u32x2 v = ldw2(r, voff, 0);
        if (__all(v[0] == tag[0] && v[1] == tag[1])) break;
        __builtin_amdgcn_s_sleep(2);
    }
    asm volatile("" ::: "memory");
}
#define RETRY(COND, LOADS)                                                   \
    if (__builtin_expect(!__all(COND), 0)) {                                 \
        _Pragma("unroll 1") for (unsigned n_ = 0; n_ < SPIN_CAP; ++n_) {     \
            __builtin_amdgcn_s_sleep(2);                                     \
            asm volatile("" ::: "memory");                                   \
            LOADS;                                                           \
            __builtin_amdgcn_s_waitcnt(0x0F70);                              \
            if (__all(COND)) break;                                          \
        }                                                                    \
    }
DEV f32x2 gelu2(f32x2 v, float a, float b, float k) {
    const f32x2 y = v * __builtin_elementwise_fma(v * v, (f32x2){b, b}, (f32x2){a, a});
    const f32x2 e = {__builtin_amdgcn_exp2f(y[0]), __builtin_amdgcn_exp2f(y[1])};
    const f32x2 d = __builtin_elementwise_fma(e, (f32x2){k, k}, (f32x2){k, k});
    return v * (f32x2){__builtin_amdgcn_rcpf(d[0]), __builtin_amdgcn_rcpf(d[1])};
}
constexpr float GC0 = -2.0f * 0.7978845608028654f * 1.4426950408889634f, GC1 = GC0 * 0.044715f;
DEV u32x4 ln_gelu_chunk(const f32x4& p, const f32x4& q, const f32x4& cp, const f32x4& cq, float mean, float a, float b, float k) {
    const f32x2 nm = {-mean, -mean};
    const f32x2 r0 = gelu2(__builtin_elementwise_fma(nm, (f32x2){cp[0], cp[1]}, (f32x2){p[0], p[1]}), a, b, k);
    const f32x2 r1 = gelu2(__builtin_elementwise_fma(nm, (f32x2){cp[2], cp[3]}, (f32x2){p[2], p[3]}), a, b, k);
    const f32x2 r2 = gelu2(__builtin_elementwise_fma(nm, (f32x2){cq[0], cq[1]}, (f32x2){q[0], q[1]}), a, b, k);
    const f32x2 r3 = gelu2(__builtin_elementwise_fma(nm, (f32x2){cq[2], cq[3]}, (f32x2){q[2], q[3]}), a, b, k);
    return (u32x4){cvtpk(r0[0], r0[1]), cvtpk(r1[0], r1[1]), cvtpk(r2[0], r2[1]), cvtpk(r3[0], r3[1])};
}

constexpr unsigned STG_A = 49152, CSL = 3 * STG_A;
struct LoadA {
    const char *a, *a3, *b;
    unsigned va[4], vb[2], da;
    u64 ta;
    unsigned tstep, scrap;
    lds_ptr xb[2], wb[4], wb1[4];
};
template <bool FO>
DEV void setupA(LoadA& L, int lane, int wid, unsigned lds0, const char* xs, const char* w0, const char* w1) {
    const int ir = 8 * wid + (lane >> 3);
    const unsigned wch = ((lane & 7) ^ (((ir >> 1) & 3) << 1)) << 4;
    if (FO) {
        L.a = L.a3 = xs; L.b = w0 - 896;
        L.va[0] = L.va[1] = (2 * wid) * 32768 + lane * 16;
        L.va[2] = L.va[3] = L.va[0] + 32768 - 2048;
        L.vb[1] = ir * (F_ * 2) + wch;
        L.vb[0] = L.vb[1] + 896;
    } else {
        const int sr = (ir & 32) | ((ir & 12) << 1) | ((ir & 16) >> 2) | (ir & 3);
        L.a = w0; L.a3 = w1 - 3072; L.b = xs;
        L.va[0] = sr * 6144 + wch; L.va[1] = L.va[0] + 1024; L.va[2] = L.va[0] + 2048; L.va[3] = sr * 2048 + wch;
#pragma unroll
        for (int i = 0; i < 2; ++i) {
            const int row = 16 * wid + 8 * i + (lane >> 3);
            L.vb[i] = row * 2048 + (((lane & 7) ^ ((row >> 1) & 7)) << 4) - i * 1024;
        }
    }
    L.da = lds0 + wid * 4096;
}
template <bool FO>
DEV void setupR(LoadA& L, char* smem, int lane, int wid) {
    const int c = lane & 15, g = lane >> 4, q = c >> 2, grp = wid >> 2;
    const int sx = (FO || grp) ? (wid & 1) : 0, sw = (2 * g + (q >> 1)) & 3;
    const lds_ptr wr = (lds_ptr)smem + ((4 * g + q) & 7) * 128 + (lane & 3) * 8;
#pragma unroll
    for (int s = 0; s < 2; ++s) {
        const int sa = s ^ sx;
        L.xb[s] = (lds_ptr)smem + (FO ? (8 * (wid & 3) + sa) * 1024 + lane * 16
                                      : 32768 + (64 * (wid & 1) + c) * 128 + (((4 * sa + g) ^ ((c >> 1) & 7)) << 4));
        PIN(L.xb[s]);
    }
#pragma unroll
    for (int nb = 0; nb < (FO ? 4 : 2); ++nb) {
        L.wb[nb] = wr + (((nb ^ (wid & 2)) ^ sw) << 5) +
                   (FO ? 32768 + grp * 1024 + (g >> 1) * 2048 + sx * 8192 : (1 - grp) * 1024 + (g >> 1) * 4096 + sx * 16384);
        L.wb1[nb] = L.wb[nb] + (FO ? 8192 : 16384) * (1 - 2 * sx);
        PIN(L.wb[nb]); PIN(L.wb1[nb]);
    }
}
#define M0SET(v) asm volatile("s_mov_b32 m0, %0\n\ts_nop 0" ::"s"(v) : "memory")
#define GL(voff, sptr, IMM, SC) asm volatile("global_load_lds_dwordx4 %0, %1 offset:" #IMM SC ::"v"(voff), "s"(sptr) : "memory")
#define A_SETA(L, so) M0SET(L.da + (so))
#define A_SETB(L, so, wid) M0SET(L.da + 32768 - (wid) * 2048 + (so))
#define A_A(L, FO, i, IMM) do { if (FO) GL(L.va[i], L.a, IMM, " sc1"); else if (i < 3) GL(L.va[i], L.a, IMM, ""); else GL(L.va[i], L.a3, IMM, ""); } while (0)
#define A_ALLA(L, FO, so) do { A_SETA(L, so); A_A(L, FO, 0, 0); A_A(L, FO, 1, 1024); A_A(L, FO, 2, 2048); A_A(L, FO, 3, 3072); A_NEXTA(L, FO); } while (0)
#define A_ALLB(L, FO, so, wid) do { A_SETB(L, so, wid); GL(L.vb[0], L.b, 0, ""); GL(L.vb[1], L.b, 1024, ""); A_NEXTB(L, FO); } while (0)
#define A_NEXTA(L, FO) do { L.a += FO ? 2048 : 64 * 6144; if (!FO) L.a3 += 64 * 2048; } while (0)
#define A_NEXTB(L, FO) L.b += FO ? 64 * F_ * 2 : 128
#define P1W 8
#define A_TOUCH(L) asm volatile("global_load_dword %0, %1, off" : "+v"(L.scrap) : "v"(L.ta) : "memory")
#define WAIT_BARX(N) WAIT_BAR(N)

template <int mode, class Hook, class Hook2>
DEV void kloopA(char* ws, char* smem, LoadA& L, int lane, int wid, f32x4 (&acc)[4][4], Hook hook, Hook2 hook2) {
    constexpr bool FO = mode == 2;
    constexpr int H2 = FO ? 4096 : 8192, NP = FO ? 4 : 2;
    float cs0 = 0.f, cs1 = 0.f, cs2 = 0.f, cs3 = 0.f;
    unsigned so2 = 2 * STG_A, tm = ~0u;
    int tc = -11;
    auto body = [&](auto kind) {
        constexpr int KIND = decltype(kind)::value;
        bf16x8 xf[4][2], wf[4][2];
#define RD(i, s)                                                                                                     \
    wf[i][s] = ldtr<H2>((s && mode ? L.wb1[i % NP] : L.wb[i % NP]) + (s && !mode ? 16384 : 0) + (i / NP) * (mode ? 3072 : 1024)); \
    xf[i][s] = ld128(L.xb[s] + i * 2048); SBAR()
        SBAR();
        if (KIND == 1) { hook(); SBAR(); }
        if (KIND == 2) { hook2(); SBAR(); }
        RD(0, 0);
        if (KIND == 0) { A_SETA(L, so2); A_A(L, FO, 0, 0); SBAR(); }
        RD(1, 0);
        if (KIND == 0) { A_A(L, FO, 1, 1024); SBAR(); }
        RD(2, 0);
        if (KIND == 0) { A_A(L, FO, 2, 2048); SBAR(); }
        RD(3, 0);
        if (KIND == 0) { A_A(L, FO, 3, 3072); SBAR(); }
        RD(0, 1);
        if (KIND == 0) { A_SETB(L, so2, wid); GL(L.vb[0], L.b, 0, ""); SBAR(); }
        RD(1, 1);
        if (KIND == 0) { GL(L.vb[1], L.b, 1024, ""); SBAR(); }
        RD(2, 1);
        if (KIND == 0 && !FO) { A_TOUCH(L); SBAR(); }
        RD(3, 1);
#undef RD
        int adv = so2 == STG_A ? -2 * (int)STG_A : (int)STG_A;
        so2 = so2 == 2 * STG_A ? 0 : so2 + STG_A;
        if (KIND == 0) { A_NEXTA(L, FO); A_NEXTB(L, FO); }
        asm volatile("" : "+s"(adv), "+s"(so2), "+s"(L.a), "+s"(L.b));
        if (!FO && KIND == 0) asm volatile("s_ashr_i32 %0, %1, 31\n\ts_add_i32 %1, %1, 1" : "=&s"(tm), "+s"(tc), "+s"(L.a3)::"scc");
        SBAR();
        if (KIND == 0) { if (FO) WAIT_BAR(6); else WAIT_BARX(P1W); } else if (KIND == 1) { __builtin_amdgcn_s_waitcnt(0x0070); BAR(); } else LG_BAR();
        if (KIND == 1 && !FO) PIN(L.scrap);
        SBAR();
#define ADV(i)                                                                                       \
    do {                                                                                             \
        if (KIND != 2) {                                                                             \
            if ((i) >= 18 && (i) < 20) { L.xb[(i) & 1] += adv; PIN(L.xb[(i) & 1]); }                 \
            if ((i) >= 20 && (i) < 20 + NP) { L.wb[(i) & 3] += adv; PIN(L.wb[(i) & 3]); }            \
            if ((i) >= 24 && (i) < 24 + NP && mode) { L.wb1[(i) & 3] += adv; PIN(L.wb1[(i) & 3]); }  \
        }                                                                                            \
        if (KIND == 0 && !FO) {                                                                      \
            if ((i) == 22) { L.tstep &= tm; PIN(L.tstep); }                                          \
            if ((i) == 23) { L.ta += L.tstep; PIN(L.ta); }                                           \
        }                                                                                            \
        SBAR();                                                                                      \
    } while (0)
        if (mode == 0) {
#pragma unroll
            for (int i = 0; i < 32; ++i) {
                if (i & 2) acc[i & 3][(i >> 2) & 3] = MFMA16(xf[(i >> 2) & 3][i >> 4], wf[i & 3][i >> 4], acc[i & 3][(i >> 2) & 3]);
                else acc[i & 3][(i >> 2) & 3] = MFMA16(wf[i & 3][i >> 4], xf[(i >> 2) & 3][i >> 4], acc[i & 3][(i >> 2) & 3]);
                ADV(i);
            }
        } else {
            const u32x4 u0 = __builtin_bit_cast(u32x4, wf[mode == 1 ? 2 : 0][0]), u1 = __builtin_bit_cast(u32x4, wf[mode == 1 ? 3 : 1][0]);
            float tmp = 0.f;
#pragma unroll
            for (int i = 0; i < 32; ++i) {
                const unsigned u = (i & 16) ? u1[(i >> 2) & 3] : u0[(i >> 2) & 3];
                if (i == 31) { cs3 += tmp; PIN(cs3); SBAR(); }
                acc[i & 3][(i >> 2) & 3] = MFMA16(wf[i & 3][i >> 4], xf[(i >> 2) & 3][i >> 4], acc[i & 3][(i >> 2) & 3]);
                if ((i & 1) == 0) { tmp = (i & 2) ? bfhi(u) : bflo(u); PIN(tmp); }
                else if (i == 31) {}
                else if (i & 16) { if (i & 2) { cs3 += tmp; PIN(cs3); } else { cs2 += tmp; PIN(cs2); } }
                else { if (i & 2) { cs1 += tmp; PIN(cs1); } else { cs0 += tmp; PIN(cs0); } }
                ADV(i);
            }
        }
#undef ADV
        if (KIND != 2) BAR();
        SBAR();
    };
#pragma unroll 1
    for (int t = 0; t < 14; ++t) body(IC<0>{});
    body(IC<1>{});
    body(IC<2>{});
    if (mode) {
        const float a = sum32(sum16(cs0 + cs1)), b = sum32(sum16(cs2 + cs3));
        LDS_AS float* csl = (LDS_AS float*)((lds_ptr)smem + CSL) + (wid >> 2) * 128 + (wid & 1) * 64 + (wid & 2) * 16 + (lane & 15);
        if (lane < 16) { csl[0] = a; csl[16] = b; }
    }
    if (wid < 4) LG_BAR();
}
template <int N>
DEV void read_cs(char* smem, int lane, int wid, f32x4 (&cs)[N]) {
    const LDS_AS float* csl = (const LDS_AS float*)((lds_ptr)smem + CSL) + (wid >> 2) * 128 + (lane >> 4) * 4;
#pragma unroll
    for (int nb = 0; nb < N; ++nb)
        cs[nb] = *(const LDS_AS f32x4*)(csl + (nb ^ (wid & 2)) * 16) + *(const LDS_AS f32x4*)(csl + 64 + (nb ^ (wid & 2)) * 16);
}

__global__ __launch_bounds__(512, 2) void k_mega(const u16* __restrict__ x, const u16* __restrict__ wqkv,
                                                 const u16* __restrict__ wo, const u16* __restrict__ w1,
                                                 const u16* __restrict__ w2, const u16* __restrict__ w3,
                                                 const u16* __restrict__ w4, const u16* __restrict__ gate_w,
                                                 char* __restrict__ ws, u16* __restrict__ out) {
    extern __shared__ __attribute__((aligned(16))) char smem[];
    const int b = blockIdx.x, xcd = b & 7, jb = b >> 3;
    const int wid = rfl(threadIdx.x >> 6);
    const unsigned lds0 = (unsigned)(uintptr_t)(lds_ptr)smem;
    const rsrc_t R = __builtin_amdgcn_make_buffer_rsrc(ws, 0, 0x7fffffff, 0x00020000);
    const int hp = 2 * xcd + (jb >> 4), rt = jb & 15;
    const int head = 2 * hp + (wid & 1), ks = wid >> 1, grp = wid >> 2;
    STAMP_INIT();
    STAMP(0);
    u32x2 tag;
    bf16x8 Q[4][2], Ka[2], Kb[2], Va[2], Vb[2], K0[2];
    unsigned fo1;
    bool fp1;
    unsigned scrap;
    {
        const int lane = fresh_lane(), c = lane & 15, g = lane >> 4, wm = wid & 1, hh = (wid >> 1) & 1, h1 = 2 * hp + hh;
        LoadA L;
        setupA<false>(L, lane, wid, lds0, (const char*)(x + (size_t)rt * 128 * D_), (const char*)(wqkv + 64 * hp),
                      (const char*)(w1 + 64 * hp));
        u32x2 ep;
        {
            const unsigned z = 0;
            asm volatile("global_load_dwordx2 %0, %1, %2 sc1" : "=v"(ep) : "v"(z), "s"(ws) : "memory");
        }
        A_ALLB(L, false, 0, wid); A_ALLA(L, false, 0);
        const int srow = rt * 128 + hp * 8 + (wid & 3) * 2;
        const char* xr = (const char*)(x + (size_t)srow * D_);
        u32x4 gx[4];
        {
            const unsigned vo = lane * 16;
            asm volatile(IF_EQ("%6", 1)
                "global_load_dwordx4 %0, %4, %5\n\tglobal_load_dwordx4 %1, %4, %5 offset:1024\n\t"
                "global_load_dwordx4 %2, %4, %5 offset:2048\n\tglobal_load_dwordx4 %3, %4, %5 offset:3072" END_IF
                : "=&v"(gx[0]), "=&v"(gx[1]), "=&v"(gx[2]), "=&v"(gx[3]) : "v"(vo), "s"(xr), "s"(grp) : "memory", "scc");
        }
        A_ALLB(L, false, STG_A, wid); A_ALLA(L, false, STG_A);
        {
            const int j = (jb * 8 + wid) * 4 + (lane >> 4), m = (j >> 1) & 3;
            const unsigned col = (2 * xcd + (j & 1)) * 128;
            if (j < 512) {
                L.ta = m < 3 ? (u64)wqkv + ((192 + (j >> 3)) * 6144 + m * 2048 + col) : (u64)w1 + ((192 + (j >> 3)) * 2048 + col);
                L.tstep = m < 3 ? 64 * 6144 : 64 * 2048;
            } else {
                L.ta = (u64)x + (((xcd & 3) * 512 + j - 512) * 2048 + 384);
                L.tstep = 128;
            }
            L.scrap = 0;
            A_TOUCH(L);
            L.ta += L.tstep;
        }
        setupR<false>(L, smem, lane, wid);
        f32x4 acc[4][4] = {};
#pragma unroll
        for (int i = 0; i < 16; ++i) PIN(acc[i >> 2][i & 3]);
        asm volatile("s_waitcnt vmcnt(11)\n\t" IF_EQ("%5", 0) "s_waitcnt vmcnt(7)" END_IF "\n\ts_barrier\n\ts_waitcnt vmcnt(7)"
        ""
                     : "+v"(ep), "+v"(gx[0]), "+v"(gx[1]), "+v"(gx[2]), "+v"(gx[3]) : "s"(grp) : "memory", "scc");
        {
            const u64 e = ((u64)(unsigned)rfl(ep[1]) << 32 | (unsigned)rfl(ep[0])) + 1;
            tag = (u32x2){(unsigned)e, (unsigned)(e >> 32)};
        }
        STAMP(1);
        const int rg = rt * 2 + wm;
        constexpr unsigned XQ = 32768;
        float mean1[4], rstd1[4];
        if (grp) {
            u32x4 rsv[4];
            const unsigned rso = WS_RS + (rg * 64 + c) * 16;
            float s1[2] = {0.f, 0.f}, s2[2] = {0.f, 0.f};
#pragma unroll
            for (int i = 0; i < 4; ++i)
#pragma unroll
                for (int e = 0; e < 4; ++e) {
                    const unsigned u = gx[i][e];
                    s1[i >> 1] = dot2(u, 0x3F803F80u, s1[i >> 1]);
                    s2[i >> 1] = dot2(u, u, s2[i >> 1]);
                }
            auto p1 = __builtin_amdgcn_permlane32_swap(__float_as_uint(s1[0]), __float_as_uint(s1[1]), false, false);
            auto p2 = __builtin_amdgcn_permlane32_swap(__float_as_uint(s2[0]), __float_as_uint(s2[1]), false, false);
            float a1 = __uint_as_float(p1[0]) + __uint_as_float(p1[1]), a2 = __uint_as_float(p2[0]) + __uint_as_float(p2[1]);
            a1 = dpp_add<0xB1>(a1); a2 = dpp_add<0xB1>(a2); a1 = dpp_add<0x4E>(a1); a2 = dpp_add<0x4E>(a2);
            a1 = dpp_add<0x141>(a1); a2 = dpp_add<0x141>(a2); a1 = dpp_add<0x140>(a1); a2 = dpp_add<0x140>(a2);
            a1 = sum16(a1); a2 = sum16(a2);
            const float mean = a1 * (1.0f / D_);
            const float rstd = __builtin_amdgcn_rsqf(__builtin_fmaxf(a2 * (1.0f / D_) - mean * mean, 0.f) + 1e-5f);
            if ((lane & 30) == 0)
                stw2(R, (srow + (lane >> 5)) * 16 + (lane & 1) * 8, WS_RS,
                     (u32x2){__float_as_uint((lane & 1) ? rstd : mean), tag[0]});
            BAR();
            kloopA<1>(ws, smem, L, lane, wid, acc, [&]() {
#pragma unroll
                for (int mb = 0; mb < 4; ++mb) rsv[mb] = ldw4(R, rso + mb * 256, 0);
            }, [&]() {
                L.ta = (u64)w2 + ((((jb & 3) * 4 + (wid & 3)) * 64 + lane) * 2048 + (8 * (xcd & 1) + (jb >> 2)) * 128);
                A_TOUCH(L);
            });
            STAMP(2);
            const float sc = 0.17677669529663687f * 1.4426950408889634f;
            LDS_AS u32x4* qd = (LDS_AS u32x4*)((lds_ptr)smem + XQ + (wid & 3) * 4096 + (g >> 1) * 1024) + (g & 1) * 32 + c;
#pragma unroll
            for (int mb = 0; mb < 4; ++mb) qd[(mb >> 1) * 128 + (mb & 1) * 16] = pack44(acc[0][mb] * sc, acc[1][mb] * sc);
            RETRY((rsv[0][1] & rsv[0][3] & rsv[1][1] & rsv[1][3] & rsv[2][1] & rsv[2][3] & rsv[3][1] & rsv[3][3]) == tag[0] &&
                      (rsv[0][1] | rsv[0][3] | rsv[1][1] | rsv[1][3] | rsv[2][1] | rsv[2][3] | rsv[3][1] | rsv[3][3]) == tag[0],
                  for (int mb = 0; mb < 4; ++mb) rsv[mb] = ldw4(R, rso + mb * 256, 0));
#pragma unroll
            for (int mb = 0; mb < 4; ++mb) { mean1[mb] = __uint_as_float(rsv[mb][0]); rstd1[mb] = __uint_as_float(rsv[mb][2]); }
            fp1 = lane == 0 || (lane == 1 && (rg == 0 || rg == 31));
            fo1 = WS_FH + h1 * 8 + (lane == 0 ? (rg + 1) * 256 : rg == 0 ? 33 * 256 : 0);
        } else {
            u32x4 gw[2];
            kloopA<0>(ws, smem, L, lane, wid, acc, [&]() {
#pragma unroll
                for (int i = 0; i < 4; ++i) gx[i] = *(const u32x4*)(xr + i * 1024 + lane * 16);
                gw[0] = *(const u32x4*)(gate_w + lane * 8);
                gw[1] = *(const u32x4*)(gate_w + 512 + lane * 8);
            }, []() {});
            STAMP(2);
            const unsigned ko = (g >> 1) * 1024 + ((g & 1) * 32 + (c >> 3) * 16 + (c & 7)) * 16;
            const unsigned vo = 2048 + (g >> 1) * 1024 + ((g & 1) * 32 + c) * 16;
            const lds_ptr kd = (lds_ptr)smem + wid * 8192;
            const unsigned kvg = WS_KV + (h1 * 64 + 2 * rg) * 4096;
#pragma unroll
            for (int i = 0; i < 4; ++i) {
                const u32x4 kk = pack44(acc[0][i], acc[1][i]);
                const u32x4 vv = pack44(acc[2 + (i & 1)][i & 2], acc[2 + (i & 1)][(i & 2) + 1]);
                stw4(R, ko + (i & 1) * 128, kvg + (i >> 1) * 4096, kk);
                stw4(R, vo + (i & 1) * 256, kvg + (i >> 1) * 4096, vv);
                *(LDS_AS u32x4*)(kd + ko + (i >> 1) * 4096 + (i & 1) * 128) = kk;
                *(LDS_AS u32x4*)(kd + vo + (i >> 1) * 4096 + (i & 1) * 256) = vv;
            }
            SBAR();
            float z[2] = {0.f, 0.f};
#pragma unroll
            for (int i = 0; i < 4; ++i)
#pragma unroll
                for (int e = 0; e < 4; ++e) {
                    const unsigned u = gx[i][e], w = gw[i & 1][e];
                    z[i >> 1] = dot2(cvtpk(bflo(u) * bflo(w), bfhi(u) * bfhi(w)), 0x3F803F80u, z[i >> 1]);
                }
            auto p1 = __builtin_amdgcn_permlane32_swap(__float_as_uint(z[0]), __float_as_uint(z[1]), false, false);
            float a1 = __uint_as_float(p1[0]) + __uint_as_float(p1[1]);
            a1 = dpp_add<0xB1>(a1); a1 = dpp_add<0x4E>(a1); a1 = dpp_add<0x141>(a1); a1 = dpp_add<0x140>(a1);
            a1 = rbf(sum16(a1));
            const float gt = rbf(1.0f / (1.0f + __expf(-a1)));
            if ((lane & 31) == 0) stw2(R, (srow + (lane >> 5)) * 8, WS_G, (u32x2){cvtpk(gt, 1.0f - gt), tag[0]});
            STAMP(22);
            post_flag(R, WS_F1 + (h1 * 32 + rg) * 8, tag, lane);
            fp1 = false;
            fo1 = 0;
        }
        STAMP(19);
        LG_BAR();
        {
            const lds_ptr kd = (lds_ptr)smem + (wid & 1) * 16384 + lane * 16, qd = (lds_ptr)smem + XQ + (wid & 1) * 8192 + lane * 16;
#pragma unroll
            for (int f = 0; f < 2; ++f) {
                K0[f] = ld128(kd + f * 1024);
#pragma unroll
                for (int i = 0; i < 4; ++i) Q[i][f] = ld128(qd + (i ^ ks) * 2048 + f * 1024);
                Ka[f] = ld128(kd + ks * 4096 + f * 1024); Va[f] = ld128(kd + ks * 4096 + 2048 + f * 1024);
            }
        }
        f32x4 cs[2];
        read_cs(smem, lane, wid, cs);
        LG_BAR();
        STAMP(3);
        if (grp) {
            const int quarter = hp >> 2;
            const int sh = quarter == 0 ? 1 : quarter == 1 ? -1 : quarter == 2 ? 2 : -2;
#pragma unroll
            for (int mb = 0; mb < 4; ++mb) {
                const float mean = mean1[mb], r = rstd1[mb];
                const float a = GC0 * r, bq = GC1 * r * r * r, k = __builtin_amdgcn_rcpf(r);
                const int t = rg * 64 + mb * 16 + c + sh, tw = t & (S_ - 1);
                u32x4 v = ln_gelu_chunk(acc[2][mb], acc[3][mb], cs[0], cs[1], mean, a, bq, k);
                if (mb == 0 || mb == 3)
                    if (t != tw) v = (u32x4){0u, 0u, 0u, 0u};
                stw4(R, (tw >> 4) * 32768 + (g * 16 + (tw & 15)) * 16, WS_HS + h1 * 1024, v);
            }
        }
        STAMP(4);
        scrap = L.scrap;
    }
    const bool p3_on = jb < 28;
    const int mt3 = 4 * (xcd & 1) + (jb & 3), nt3 = 7 * (xcd >> 1) + (jb >> 2);
    LoadA L3;
    float mean3[4], rstd3[4], omg3[4];
    {
        int lane = fresh_lane();
        const unsigned kvh = WS_KV + head * 64 * 4096;
        unsigned vl = lane * 16;
        int kt = rt >> 1;
#define KVO(t) (kvh + ((t) * 8 + 2 * ks) * 4096)
#define LDF(so, imm) BC8(ldw4(R, vl + (imm), so))
        float mref = -INFINITY;
#pragma unroll
        for (int i = 0; i < 4; ++i) {
            f32x16 s = MFMA32(K0[0], Q[i][0], (f32x16){});
            s = MFMA32(K0[1], Q[i][1], s);
#pragma unroll
            for (int r = 0; r < 16; r += 2) mref = __builtin_fmaxf(__builtin_fmaxf(s[r], s[r + 1]), mref);
        }
        mref = max32(mref);
        f32x16 O[4] = {};
        f32x4 Ls = {};
#define EX(X, i) X[i] = __builtin_amdgcn_exp2f(X[i])
#define CV(P, X, k) P[(k) >> 2][(k) & 3] = cvtpk(X[2 * (k)], X[2 * (k) + 1])
#define GRP4(Sc, Pc, j) EX(Sc, 4 * (j)); EX(Sc, 4 * (j) + 1); EX(Sc, 4 * (j) + 2); PIN(Sc); SBAR()
#define GRP4B(Sc, Pc, j) EX(Sc, 4 * (j) + 3); PIN(Sc); SBAR(); CV(Pc, Sc, 2 * (j)); PIN(Pc[(j) >> 1]); SBAR(); CV(Pc, Sc, 2 * (j) + 1); PIN(Pc[(j) >> 1]); SBAR()
#define STEP(Sc, Sn, Pc, Pp, Kn, qn, Vp, bp)                                                                    \
    do {                                                                                                        \
        SBAR();                                                                                                 \
        Sn = MFMA32(Kn[0], Q[qn][0], C); PIN(Sn); SBAR(); GRP4(Sc, Pc, 0); GRP4B(Sc, Pc, 0);                    \
        O[bp] = MFMA32(Vp[0], BC8(Pp[0]), O[bp]); PIN(O[bp]); SBAR(); GRP4(Sc, Pc, 1);                          \
        Ls = MFMA16(sel[bp], BC8(Pp[0]), Ls); PIN(Ls); SBAR(); GRP4B(Sc, Pc, 1);                                \
        Sn = MFMA32(Kn[1], Q[qn][1], Sn); PIN(Sn); SBAR(); GRP4(Sc, Pc, 2); GRP4B(Sc, Pc, 2);                   \
        O[bp] = MFMA32(Vp[1], BC8(Pp[1]), O[bp]); PIN(O[bp]); SBAR(); GRP4(Sc, Pc, 3);                          \
        Ls = MFMA16(sel[bp], BC8(Pp[1]), Ls); PIN(Ls); SBAR(); GRP4B(Sc, Pc, 3);                                \
    } while (0)
        const char* t3 = (const char*)w3 + (b * 4 + (wid & 3)) * 1024;
#define TCH3() do { GLDS1(lds0 + 130 * 1024, vl, t3); t3 += 1 << 20; } while (0)
#define ATT_INIT()                                                                                              \
        bf16x8 sel[4];                                                                                          \
        _Pragma("unroll") for (int i = 0; i < 4; ++i) {                                                         \
            const short v = ((lane & 3) == i && ((lane >> 2) & 1) == ((lane >> 4) & 1)) ? (short)0x3F80 : (short)0; \
            sel[i] = (bf16x8){v, v, v, v, v, v, v, v};                                                          \
            PIN(sel[i]);                                                                                        \
        }                                                                                                       \
        f32x16 C, SA, SB;                                                                                       \
        _Pragma("unroll") for (int r = 0; r < 16; ++r) C[r] = -mref;                                            \
        PIN(C);                                                                                                 \
        u32x4 PA[2] = {}, PB[2] = {};                                                                           \
        SA = MFMA32(Ka[0], Q[0][0], C);                                                                         \
        SA = MFMA32(Ka[1], Q[0][1], SA)
#define ATT_FLAG(fl, fa) if (!__all(fl[0] == tag[0] && fl[1] == tag[1])) wait_flags(R, fa, tag)
#define ATT_RUN(nit, TOP, MID, LASTV)                                                                           \
        _Pragma("unroll 1") for (int it = 0; it < (nit); ++it) {                                                \
            const bool last = it == (nit) - 1;                                                                  \
            const unsigned kvo = KVO(kt);                                                                       \
            kt = (kt + 1) & 7;                                                                                  \
            const unsigned n2 = last ? kvo : KVO(kt), nv = last ? (LASTV) : n2, fa = WS_F1 + (head * 32 + kt * 4 + ks) * 8; \
            TOP;                                                                                                \
            const u32x2 fl = ldw2(R, 0, fa);                                                                    \
            STEP(SA, SB, PA, PB, Ka, 1, Vb, 3);                                                                 \
            Vb[0] = LDF(kvo + 4096, 2048); Vb[1] = LDF(kvo + 4096, 3072);                                       \
            TCH3();                                                                                             \
            STEP(SB, SA, PB, PA, Ka, 2, Va, 0);                                                                 \
            MID;                                                                                                \
            STEP(SA, SB, PA, PB, Ka, 3, Va, 1);                                                                 \
            ATT_FLAG(fl, fa);                                                                                   \
            Ka[0] = LDF(n2, 0); Ka[1] = LDF(n2, 1024);                                                          \
            STEP(SB, SA, PB, PA, Kb, 0, Va, 2);                                                                 \
            MID;                                                                                                \
            STEP(SA, SB, PA, PB, Kb, 1, Va, 3);                                                                 \
            Va[0] = LDF(nv, 2048); Va[1] = LDF(nv, 3072);                                                       \
            STEP(SB, SA, PB, PA, Kb, 2, Vb, 0);                                                                 \
            MID;                                                                                                \
            STEP(SA, SB, PA, PB, Kb, 3, Vb, 1);                                                                 \
            Kb[0] = LDF(n2 + 4096, 0); Kb[1] = LDF(n2 + 4096, 1024);                                            \
            STEP(SB, SA, PB, PA, Ka, 0, Vb, 2);                                                                 \
            MID;                                                                                                \
        }                                                                                                       \
        O[3] = MFMA32(Vb[0], BC8(PB[0]), O[3]);                                                                 \
        O[3] = MFMA32(Vb[1], BC8(PB[1]), O[3]);                                                                 \
        Ls = MFMA16(sel[3], BC8(PB[0]), Ls);                                                                    \
        Ls = MFMA16(sel[3], BC8(PB[1]), Ls)
        const int mt2 = 4 * (xcd >> 1) + (jb & 3), nt2 = 8 * (xcd & 1) + (jb >> 2);
        const int n1 = grp ? 5 : 3;
        {
            const int ir = lane >> 3;
            const unsigned wv = ir * 2048 + (((lane & 7) ^ (((ir >> 1) & 3) << 1)) << 4);
            const char* wp = (const char*)(w2 + (size_t)(128 * wid) * D_ + nt2 * 64);
            unsigned wd = lds0 + wid * 16384;
#define W2L() do { GLDS(wd, wv, wp, 0, ""); wd += 1024; wp += 16384; } while (0)
            STAMP(5);
            Vb[0] = Vb[1] = (bf16x8){};
            ATT_INIT();
            {
                kt = (kt + 1) & 7;
                const unsigned nb = kvh + ((rt ^ 1) * 4 + ks) * 4096, n2 = KVO(kt);
                const unsigned fa = WS_F1 + (head * 32 + (lane < 32 ? (rt ^ 1) * 2 + (ks >> 1) : kt * 4 + ks)) * 8;
                STEP(SA, SB, PA, PB, Ka, 1, Vb, 3);
                STAMP(20);
                wait_flags(R, fa, tag);
                asm volatile("s_waitcnt vmcnt(0)" : "+v"(scrap)::"memory");
                if (fp1) stw2(R, fo1, 0, tag);
                STAMP(21);
                Kb[0] = LDF(nb, 0); Kb[1] = LDF(nb, 1024);
                Vb[0] = LDF(nb, 2048); Vb[1] = LDF(nb, 3072);
                STEP(SB, SA, PB, PA, Ka, 2, Va, 0);
                STEP(SA, SB, PA, PB, Ka, 3, Va, 1);
                Ka[0] = LDF(n2, 0); Ka[1] = LDF(n2, 1024);
                STEP(SB, SA, PB, PA, Kb, 0, Va, 2);
                STEP(SA, SB, PA, PB, Kb, 1, Va, 3);
                Va[0] = LDF(n2, 2048); Va[1] = LDF(n2, 3072);
                STEP(SB, SA, PB, PA, Kb, 2, Vb, 0);
                W2L(); W2L();
                STEP(SA, SB, PA, PB, Kb, 3, Vb, 1);
                Kb[0] = LDF(n2 + 4096, 0); Kb[1] = LDF(n2 + 4096, 1024);
                STEP(SB, SA, PB, PA, Ka, 0, Vb, 2);
                W2L(); W2L();
            }
            STAMP(18);
            ATT_RUN(n1 - 1, if (it == 2) BAR(), if (it == 0) { W2L(); W2L(); W2L(); }, kvo);
            if (!grp) BAR();
            STAMP(6);
        }
        {
            lane = fresh_lane();
            vl = lane * 16;
            const int c = lane & 15, g = lane >> 4, q = c >> 2, mb = mt2 * 8 + wid;
            wait_flags(R, WS_FH + (((mb * 16 - 2) >> 6) + 1) * 256 + lane * 8, tag);
            STAMP(24);
            const unsigned hso = WS_HS + mb * 32768;
            bf16x8 X[16];
#pragma unroll
            for (int i = 0; i < 16; ++i) X[i] = LDF(hso + (i >> 2) * 4096, (i & 3) * 1024);
            u32x4 xr[2];
#pragma unroll
            for (int p = 0; p < 2; ++p)
                xr[p] = *(const u32x4*)(x + (size_t)(mb * 16 + c) * D_ + nt2 * 64 + p * 32 + (g & 1) * 16 + (g >> 1) * 8);
            lds_ptr wb[4];
#pragma unroll
            for (int nb = 0; nb < 4; ++nb)
                wb[nb] = (lds_ptr)smem + (4 * g + q) * 128 + ((nb ^ ((2 * g + (q >> 1)) & 3)) << 5) + (lane & 3) * 8;
            f32x4 a2[4] = {};
            bf16x8 W[2][4];
#pragma unroll
            for (int nb = 0; nb < 4; ++nb) W[0][nb] = ldtr(wb[nb]);
#pragma unroll
            for (int kb = 0; kb < 32; ++kb) {
                if (kb == 15)
#pragma unroll
                    for (int nb = 0; nb < 4; ++nb) { wb[nb] += 65536; PIN(wb[nb]); }
                if (kb < 31)
#pragma unroll
                    for (int nb = 0; nb < 4; ++nb) W[(kb + 1) & 1][nb] = ldtr(wb[nb] + ((kb + 1) & 15) * 4096);
                SBAR();
#pragma unroll
                for (int nb = 0; nb < 4; ++nb) a2[nb] = MFMA16(W[kb & 1][nb], X[kb & 15], a2[nb]);
                SBAR();
                if (kb < 16) X[kb] = LDF(hso + ((kb + 16) >> 2) * 4096, (kb & 3) * 1024);
                if (kb == 20) { Ka[0] = LDF(KVO(kt), 0); Ka[1] = LDF(KVO(kt), 1024); Va[0] = LDF(KVO(kt), 2048); Va[1] = LDF(KVO(kt), 3072); }
            }
            float s1 = 0.f, s2 = 0.f;
#pragma unroll
            for (int p = 0; p < 2; ++p) {
                auto u = __builtin_amdgcn_permlane16_swap(xr[p][0], xr[p][2], false, false);
                auto v = __builtin_amdgcn_permlane16_swap(xr[p][1], xr[p][3], false, false);
                const f32x4 lo = {bflo(u[0]), bfhi(u[0]), bflo(v[0]), bfhi(v[0])}, hi = {bflo(u[1]), bfhi(u[1]), bflo(v[1]), bfhi(v[1])};
                const u32x4 o = pack44(a2[2 * p] + lo, a2[2 * p + 1] + hi);
#pragma unroll
                for (int e = 0; e < 4; ++e) { const unsigned w = o[e]; s1 = dot2(w, 0x3F803F80u, s1); s2 = dot2(w, w, s2); }
                stw4(R, vl, WS_X1 + (mb * 32 + nt2 * 2 + p) * 1024, o);
            }
            s1 = sum32(sum16(s1)); s2 = sum32(sum16(s2));
            if (g == 0) stw2(R, c * 8, WS_ST + (mb * 16 + nt2) * 128, (u32x2){__float_as_uint(s1), __float_as_uint(s2)});
            post_flag(R, WS_F2 + (mt2 >> 1) * 2048 + (((mt2 & 1) * 16 + nt2) * 8 + wid) * 8, tag, lane);
            STAMP(7);
        }
        setupA<true>(L3, lane, wid, lds0, ws + WS_X1 + (size_t)mt3 * 16 * 32768, (const char*)(w3 + nt3 * 128), nullptr);
        const unsigned f2o = WS_F2 + mt3 * 2048;
        {
            Kb[0] = LDF(KVO(kt) + 4096, 0); Kb[1] = LDF(KVO(kt) + 4096, 1024);
            Vb[0] = Vb[1] = (bf16x8){};
            ATT_INIT();
            ATT_RUN(8 - n1, if (last) BAR(), , f2o - 2048);
        }
        STAMP(8);
#undef ATT_RUN
#undef ATT_INIT
#undef ATT_FLAG
#undef W2L
#undef TCH3
#undef STEP
#undef GRP4
#undef GRP4B
        const int blk = rt * 4 + ks;
        u32x2 gv = ldw2(R, (blk * 32 + (lane & 31)) * 8, WS_G);
        u32x2 stp[4][4], og[4];
        if (p3_on) {
            A_ALLB(L3, true, 0, wid);
            const u32x4 f0 = __builtin_bit_cast(u32x4, Va[0]), f1 = __builtin_bit_cast(u32x4, Va[1]);
            if (!__all(f0[0] == tag[0] && f0[1] == tag[1] && f0[2] == tag[0] && f0[3] == tag[1] && f1[0] == tag[0] &&
                       f1[1] == tag[1] && f1[2] == tag[0] && f1[3] == tag[1])) {
#pragma unroll 1
                for (int i = 0; i < 4; ++i) wait_flags(R, f2o + i * 512 + lane * 8, tag);
            }
            A_ALLA(L3, true, 0); A_ALLA(L3, true, STG_A);
#pragma unroll
            for (int mb = 0; mb < 4; ++mb) {
                const int mblk = mt3 * 16 + (wid & 3) * 4 + mb;
#pragma unroll
                for (int i = 0; i < 4; ++i) stp[mb][i] = ldw2(R, (lane >> 4) * 512 + (lane & 15) * 8 + i * 128, WS_ST + mblk * 2048);
                og[mb] = ldw2(R, (lane & 15) * 8, WS_G + mblk * 128);
            }
        }
        constexpr unsigned CMB = 88 * 1024;
        f32x16 o;
        float l;
        {
            LDS_AS f32x4* me = (LDS_AS f32x4*)((lds_ptr)smem + CMB + wid * 8192) + lane;
            LDS_AS f32x2* ml = (LDS_AS f32x2*)((lds_ptr)smem + CMB + 65536 + wid * 512) + lane;
#pragma unroll
            for (int i = 0; i < 2; ++i)
#pragma unroll
                for (int e = 0; e < 4; ++e)
                    me[(i * 4 + e) * 64] = (f32x4){O[2 + i][4 * e], O[2 + i][4 * e + 1], O[2 + i][4 * e + 2], O[2 + i][4 * e + 3]};
            *ml = (f32x2){Ls[2], Ls[3]};
            LG_BAR();
            LDS_AS f32x4* pa = (LDS_AS f32x4*)((lds_ptr)smem + CMB + (wid ^ 4) * 8192) + lane;
#pragma unroll
            for (int i = 0; i < 2; ++i)
#pragma unroll
                for (int e = 0; e < 4; ++e) {
                    const f32x4 v = pa[(i * 4 + e) * 64];
#pragma unroll
                    for (int j = 0; j < 4; ++j) O[i][4 * e + j] += v[j];
                }
            const f32x2 l2 = *((const LDS_AS f32x2*)((lds_ptr)smem + CMB + 65536 + (wid ^ 4) * 512) + lane) + (f32x2){Ls[0], Ls[1]};
#pragma unroll
            for (int e = 0; e < 4; ++e) pa[e * 64] = (f32x4){O[1][4 * e], O[1][4 * e + 1], O[1][4 * e + 2], O[1][4 * e + 3]};
            ((LDS_AS float*)(pa + 256 - lane))[lane] = l2[1];
            LG_BAR();
            const LDS_AS f32x4* p2 = (const LDS_AS f32x4*)((lds_ptr)smem + CMB + (wid ^ 6) * 8192) + lane;
#pragma unroll
            for (int e = 0; e < 4; ++e) {
                const f32x4 v = p2[e * 64];
#pragma unroll
                for (int j = 0; j < 4; ++j) o[4 * e + j] = O[0][4 * e + j] + v[j];
            }
            l = ((const LDS_AS float*)(p2 + 256 - lane))[lane] + l2[0];
            LG_BAR();
        }
        if (p3_on) A_ALLB(L3, true, STG_A, wid);
        {
            float n = 0.f;
#pragma unroll
            for (int r = 0; r < 16; ++r) n = __builtin_fmaf(o[r], 0.f, n);
            const bool ok = l >= 0x1p-50f && l <= 0x1p50f && n == 0.f;
            if (__builtin_expect(!__all(ok), 0)) {
                const bf16x8 q0 = Q[0][0], q1 = Q[0][1];
                wait_flags(R, WS_F1 + (head * 32 + (lane & 31)) * 8, tag);
                float m = -INFINITY;
#pragma unroll 1
                for (int t = 0; t < 64; ++t) {
                    f32x16 s = MFMA32(LDF(kvh + t * 4096, 0), q0, (f32x16){});
                    s = MFMA32(LDF(kvh + t * 4096, 1024), q1, s);
#pragma unroll
                    for (int r = 0; r < 16; ++r) m = __builtin_fmaxf(m, s[r]);
                }
                m = max32(m);
                o = (f32x16){};
                l = 0.f;
#pragma unroll 1
                for (int t = 0; t < 64; ++t) {
                    f32x16 s = MFMA32(LDF(kvh + t * 4096, 0), q0, (f32x16){});
                    s = MFMA32(LDF(kvh + t * 4096, 1024), q1, s);
                    u32x4 p[2];
#pragma unroll
                    for (int r = 0; r < 8; ++r) {
                        const unsigned w = cvtpk(__builtin_amdgcn_exp2f(s[2 * r] - m), __builtin_amdgcn_exp2f(s[2 * r + 1] - m));
                        p[r >> 2][r & 3] = w;
                        l += bflo(w) + bfhi(w);
                    }
                    o = MFMA32(LDF(kvh + t * 4096, 2048), BC8(p[0]), o);
                    o = MFMA32(LDF(kvh + t * 4096, 3072), BC8(p[1]), o);
                }
                l = sum32(l);
            }
        }
#pragma unroll 1
        for (unsigned n = 0; n < SPIN_CAP; ++n) {
            if (__all(gv[1] == tag[0])) break;
            __builtin_amdgcn_s_sleep(2);
            asm volatile("" ::: "memory");
            gv = ldw2(R, (blk * 32 + (lane & 31)) * 8, WS_G);
        }
        {
            const float sc = bflo(gv[0]) / l;
            const unsigned vo = ((lane >> 4) & 1) * (144 * 1024) + ((lane >> 5) * 16 + (lane & 15)) * 16;
#pragma unroll
            for (int u = 0; u < 2; ++u)
                stw4(R, vo + u * 512, WS_A4 + (blk * 2 * 144 + (head >> 4) * 72 + (head & 15)) * 1024,
                     pack44((f32x4){o[4 * u], o[4 * u + 1], o[4 * u + 2], o[4 * u + 3]} * sc,
                            (f32x4){o[8 + 4 * u], o[9 + 4 * u], o[10 + 4 * u], o[11 + 4 * u]} * sc));
        }
        if (p3_on) {
#pragma unroll 1
            for (unsigned n = 0; n < SPIN_CAP; ++n) {
                if (__all(og[0][1] == tag[0] && og[1][1] == tag[0] && og[2][1] == tag[0] && og[3][1] == tag[0])) break;
                __builtin_amdgcn_s_sleep(2);
                asm volatile("" ::: "memory");
#pragma unroll
                for (int mb = 0; mb < 4; ++mb) og[mb] = ldw2(R, (lane & 15) * 8, WS_G + (mt3 * 16 + (wid & 3) * 4 + mb) * 128);
            }
#pragma unroll
            for (int mb = 0; mb < 4; ++mb) {
                float s1 = 0.f, s2 = 0.f;
#pragma unroll
                for (int i = 0; i < 4; ++i) { s1 += __uint_as_float(stp[mb][i][0]); s2 += __uint_as_float(stp[mb][i][1]); }
                s1 = sum32(sum16(s1)); s2 = sum32(sum16(s2));
                mean3[mb] = s1 * (1.0f / D_);
                rstd3[mb] = __builtin_amdgcn_rsqf(__builtin_fmaxf(s2 * (1.0f / D_) - mean3[mb] * mean3[mb], 0.f) + 1e-5f);
                omg3[mb] = bfhi(og[mb][0]);
            }
        }
        post_flag(R, WS_FO + (((blk >> 2) * 2 + (head >> 4)) * 64 + (head & 15) * 4 + (blk & 3)) * 8, tag, lane);
        STAMP(9);
#undef LDF
#undef KVO
#undef EX
#undef CV
    }
    {
        const int lane = fresh_lane(), c = lane & 15, g = lane >> 4, q = c >> 2;
        const int kh = xcd & 1, mt4 = 8 * ((xcd >> 1) & 1) + (jb & 7), nt4 = 4 * (xcd >> 2) + (jb >> 3);
        const int j = wid & 3, wm = j & 1, wn = j >> 1;
        constexpr unsigned STG_B = 32768;
        const unsigned xv0 = (2 * (j ^ grp ^ 1) + kh) * (144 * 1024) + lane * 16 + 2048;
        const unsigned xv1 = (2 * (j ^ grp ^ 1) + (kh ^ 1)) * (144 * 1024) + lane * 16;
        const int ir = 16 * j + (lane >> 3);
        const unsigned wva = ir * 2048 + (((lane & 7) ^ (((ir >> 1) & 3) << 1)) << 4), wvb = wva + 8 * 2048 - 1024;
        const char* xs4 = ws + WS_A4 + ((size_t)mt4 * 8 * 144 + kh * 72) * 1024 - 2048;
        const char* wo4 = (const char*)(wo + (size_t)kh * 512 * D_ + nt4 * 128);
        const char* w44 = (const char*)(w4 + (size_t)kh * 1792 * D_ + nt4 * 128) - 8 * 131072;
        const unsigned dx4 = lds0 + j * 4096, dw4 = lds0 + 16384 + j * 2048;
#define B_SET(tile)                                                                          \
    const int tl_ = (tile);                                                                  \
    const char* bx_ = xs4 + tl_ * 2048;                                                      \
    const char* bw_ = (tl_ < 8 ? wo4 : w44) + tl_ * 131072;                                  \
    const char* bw1_ = bw_ + 128;                                                            \
    const unsigned bo_ = (tl_ & 3) * STG_B
#define B_X0() GLDS(dx4 + bo_, xv0, bx_, 0, " sc1")
#define B_X1() GLDS(dx4 + bo_, xv0, bx_, 1024, " sc1")
#define B_X2() GLDS(dx4 + bo_, xv1, bx_, 2048, " sc1")
#define B_X3() GLDS(dx4 + bo_, xv1, bx_, 3072, " sc1")
#define B_W0() GLDS(dw4 + bo_, wva, bw_, 0, "")
#define B_W1() GLDS(dw4 + bo_, wvb, bw_, 1024, "")
#define B_W2() GLDS(dw4 + 8192 + bo_, wva, bw1_, 0, "")
#define B_W3() GLDS(dw4 + 8192 + bo_, wvb, bw1_, 1024, "")
#define B_ALL(tile) do { B_SET(tile); B_W0(); B_W1(); B_W2(); B_W3(); B_X0(); B_X1(); B_X2(); B_X3(); } while (0)
        const unsigned foo = WS_FO + (mt4 * 2 + kh) * 512 + lane * 8;
        if (p3_on) {
            setupR<true>(L3, smem, lane, wid);
            f32x4 acc[4][4] = {};
#pragma unroll
            for (int i = 0; i < 16; ++i) PIN(acc[i >> 2][i & 3]);
            BAR();
            if (grp) BAR();
            STAMP(10);
            u32x2 fo;
            kloopA<2>(ws, smem, L3, lane, wid, acc, []() {}, [&]() { fo = ldw2(R, foo, 0); });
            STAMP(11);
            if (grp) LG_BAR();
            if (!__all(fo[0] == tag[0] && fo[1] == tag[1])) wait_flags(R, foo, tag);
            if (grp) { B_ALL(0); B_ALL(2); } else { B_ALL(1); }
            f32x4 cs[4];
            read_cs(smem, lane, wid, cs);
            const unsigned a4o = WS_A4 + ((mt3 * 16 + j * 4) * 144 + (nt3 / 14) * 72 + 16 + (nt3 % 14) * 4 + grp * 2) * 1024;
#pragma unroll
            for (int mb = 0; mb < 4; ++mb) {
                const float r = rstd3[mb], a = GC0 * r, bq = GC1 * r * r * r;
                const float k = __builtin_fminf(__builtin_amdgcn_rcpf(r * omg3[mb]), 1e30f);
#pragma unroll
                for (int p = 0; p < 2; ++p)
                    stw4(R, lane * 16, a4o + (mb * 144 + (p ^ (j >> 1))) * 1024,
                         ln_gelu_chunk(acc[2 * p][mb], acc[2 * p + 1][mb], cs[2 * p], cs[2 * p + 1], mean3[mb], a, bq, k));
                if (mb == 0) { SBAR(); if (!grp) BAR(); SBAR(); }
            }
            post_flag(R, WS_F3 + (((mt3 * 2 + (j >> 1)) * 2 + nt3 / 14) * 64 + (nt3 % 14) * 4 + grp * 2 + (j & 1)) * 8, tag, lane);
        } else {
            const int idx = (xcd * 4 + jb - 28) * 8 + wid;
            unsigned scrap = 0;
            asm volatile("global_load_dword %0, %1, %2" : "+v"(scrap) : "v"((idx * 64 + lane) * 128), "s"(wo) : "memory");
#pragma unroll 1
            for (int i = 0; i < 4; ++i) {
                __builtin_amdgcn_s_sleep(24);
                const unsigned vo = ((((i & 1) * 2 + (i >> 1)) * 256 + idx) * 56 + (lane < 56 ? lane : 55)) * 128;
                asm volatile("global_load_dword %0, %1, %2" : "+v"(scrap) : "v"(vo), "s"(w4) : "memory");
            }
            asm volatile("s_waitcnt vmcnt(0)" : "+v"(scrap)::"memory");
            wait_flags(R, foo, tag);
            if (grp) { B_ALL(0); B_ALL(2); } else { B_ALL(1); }
            DRAIN();
        }
        STAMP(12);
        BAR();
        if (grp) BAR();
        STAMP(13);
        lds_ptr xb = (lds_ptr)smem + grp * STG_B + (8 * wm) * 1024 + lane * 16, wb[4];
#pragma unroll
        for (int nb = 0; nb < 4; ++nb)
            wb[nb] = (lds_ptr)smem + grp * STG_B + 16384 + wn * 8192 + (4 * g + q) * 128 + ((nb ^ ((2 * g + (q >> 1)) & 3)) << 5) + (lane & 3) * 8;
        f32x4 acc[4][4] = {};
        const unsigned f3o = WS_F3 + (mt4 * 2 + kh) * 512 + (lane < 56 ? lane : 0) * 8;
        const int mblk = mt4 * 8 + wm * 4 + grp * 2 + kh;
        const unsigned go = WS_G + mblk * 128 + c * 8;
        const char *bx = xs4 + (3 + grp) * 2048, *bw = wo4 + (3 + grp) * 131072;
        unsigned bo = (3 - 3 * grp) * STG_B;
        int adv = 2 * STG_B;
        u32x2 f3 = {0u, 0u};
        auto body = [&](auto kind) {
            constexpr int KIND = decltype(kind)::value;
            bf16x8 xf[4][2], wf[4][2];
            const char* bw1 = bw + 128;
            SBAR();
            if (KIND == 0) asm volatile("global_load_dwordx2 %0, %1, %2 sc1" : "+v"(f3) : "v"(f3o), "s"(ws) : "memory");
            wf[0][0] = ldtr(wb[0]); xf[0][0] = ld128(xb); SBAR();
            if (KIND == 0) { M0SET(dx4 + bo); GL(xv0, bx, 0, " sc1"); SBAR(); }
            wf[1][0] = ldtr(wb[1]); xf[1][0] = ld128(xb + 2048); SBAR();
            if (KIND == 0) { GL(xv0, bx, 1024, " sc1"); SBAR(); }
            wf[2][0] = ldtr(wb[2]); xf[2][0] = ld128(xb + 4096); SBAR();
            if (KIND == 0) { GL(xv1, bx, 2048, " sc1"); SBAR(); }
            wf[3][0] = ldtr(wb[3]); xf[3][0] = ld128(xb + 6144); SBAR();
            if (KIND == 0) { GL(xv1, bx, 3072, " sc1"); SBAR(); }
            wf[0][1] = ldtr(wb[0] + 4096); xf[0][1] = ld128(xb + 1024); SBAR();
            if (KIND == 0) { M0SET(dw4 + bo); GL(wva, bw, 0, ""); SBAR(); }
            wf[1][1] = ldtr(wb[1] + 4096); xf[1][1] = ld128(xb + 3072); SBAR();
            if (KIND == 0) { GL(wvb, bw, 1024, ""); SBAR(); }
            wf[2][1] = ldtr(wb[2] + 4096); xf[2][1] = ld128(xb + 5120); SBAR();
            if (KIND == 0) { M0SET(dw4 + 8192 + bo); GL(wva, bw1, 0, ""); SBAR(); }
            wf[3][1] = ldtr(wb[3] + 4096); xf[3][1] = ld128(xb + 7168); SBAR();
            if (KIND == 0) {
                GL(wvb, bw1, 1024, "");
                bx += 4096; bw += 2 * 131072; bo ^= 2 * STG_B;
                asm volatile("" : "+s"(bx), "+s"(bw), "+s"(bo));
                SBAR();
                WAIT_BAR(9);
            } else {
                WAIT_BAR(0);
            }
            SBAR();
#pragma unroll
            for (int i = 0; i < 32; ++i) {
                acc[i & 3][(i >> 2) & 3] = MFMA16(wf[i & 3][i >> 4], xf[(i >> 2) & 3][i >> 4], acc[i & 3][(i >> 2) & 3]);
                if (i == 19) { xb += adv; PIN(xb); }
                if (i >= 20 && i < 24) { wb[i & 3] += adv; PIN(wb[i & 3]); }
                SBAR();
            }
            if (KIND == 0) BAR();
            SBAR();
            adv = -adv;
        };
#pragma unroll 1
        for (int seg = 0, n = 3 - grp; seg < 2; ++seg, n = 14) {
#pragma unroll 1
            for (int t = 0; t < n; ++t) {
                body(IC<0>{});
            }
            asm volatile("s_waitcnt vmcnt(8)" : "+v"(f3)::"memory");
            if (seg == 0) {
                if (!__all(f3[0] == tag[0] && f3[1] == tag[1])) wait_flags(R, f3o, tag);
                bw = w44 + (9 - grp) * 131072;
            }
        }
#pragma unroll 1
        for (int t = 0; t <= grp; ++t) {
            body(IC<1>{});
            if (t == 0) BAR();
        }
        u32x4 xr[2];
        xr[0] = ldw4(R, lane * 16, WS_X1 + (mblk * 32 + nt4 * 4 + wn * 2) * 1024);
        xr[1] = ldw4(R, lane * 16 + 1024, WS_X1 + (mblk * 32 + nt4 * 4 + wn * 2) * 1024);
        u32x2 og = ldw2(R, go, 0);
        STAMP(14);
        f32x4 res[4][2];
        {
            LDS_AS f32x4* ex = (LDS_AS f32x4*)((lds_ptr)smem + wid * 8192) + lane;
            const LDS_AS f32x4* ey = (const LDS_AS f32x4*)((lds_ptr)smem + (wid ^ 4) * 8192) + lane;
#pragma unroll
            for (int i = 0; i < 8; ++i) ex[i * 64] = acc[i & 3][2 + (i >> 2)];
            LG_BAR();
#pragma unroll
            for (int i = 0; i < 8; ++i) res[i & 3][i >> 2] = ey[i * 64] + acc[i & 3][i >> 2];
        }
        const int me = (mt4 * 8 + nt4) * 2 + kh;
#pragma unroll
        for (int nb = 0; nb < 4; ++nb)
            stw4(R, lane * 16 + nb * 1024, WS_EX + me * 32768 + wid * 4096, __builtin_bit_cast(u32x4, res[nb][1]));
        post_flag(R, WS_FX + (me * 8 + wid) * 8, tag, lane);
        STAMP(15);
        wait_flags(R, WS_FX + ((me ^ 1) * 8 + wid) * 8, tag);
        STAMP(16);
        u32x4 pv[4];
#pragma unroll
        for (int nb = 0; nb < 4; ++nb) pv[nb] = ldw4(R, lane * 16 + nb * 1024, WS_EX + (me ^ 1) * 32768 + wid * 4096);
#pragma unroll 1
        for (unsigned n = 0; n < SPIN_CAP; ++n) {
            if (__all(og[1] == tag[0])) break;
            __builtin_amdgcn_s_sleep(2);
            asm volatile("" ::: "memory");
            og = ldw2(R, go, 0);
        }
        const float omg = bfhi(og[0]);
#pragma unroll
        for (int p = 0; p < 2; ++p) {
            const f32x4 lo = {bflo(xr[p][0]), bfhi(xr[p][0]), bflo(xr[p][1]), bfhi(xr[p][1])};
            const f32x4 hi = {bflo(xr[p][2]), bfhi(xr[p][2]), bflo(xr[p][3]), bfhi(xr[p][3])};
            const u32x4 o = pack44(lo * omg + (res[2 * p][0] + __builtin_bit_cast(f32x4, pv[2 * p])),
                                   hi * omg + (res[2 * p + 1][0] + __builtin_bit_cast(f32x4, pv[2 * p + 1])));
            auto u = __builtin_amdgcn_permlane16_swap(o[0], o[2], false, false);
            auto v = __builtin_amdgcn_permlane16_swap(o[1], o[3], false, false);
            *(u32x4*)(out + (size_t)(mblk * 16 + c) * D_ + nt4 * 128 + wn * 64 + p * 32 + (g & 1) * 16 + (g >> 1) * 8) =
                (u32x4){u[0], v[0], u[1], v[1]};
        }
        if (b == 0 && threadIdx.x == 0) stw2(R, 0, WS_EPOCH, tag);
        STAMP(17);
        STAMP_OUT();
    }
}

extern "C" void kernel_launch(void* const* d_in, const int* in_sizes, int n_in, void* d_out, int out_size, void* d_ws,
                              size_t ws_size, hipStream_t stream) {
    (void)hipFuncSetAttribute((const void*)k_mega, hipFuncAttributeMaxDynamicSharedMemorySize, LDS_BYTES);
    k_mega<<<256, 512, LDS_BYTES, stream>>>((const u16*)d_in[0], (const u16*)d_in[1], (const u16*)d_in[2], (const u16*)d_in[4],
                                             (const u16*)d_in[5], (const u16*)d_in[6], (const u16*)d_in[7], (const u16*)d_in[8],
                                             (char*)d_ws, (u16*)d_out);
}
```

```cpp
#include <hip/hip_runtime.h>
#include <hip/hip_bf16.h>
#include <stdint.h>

typedef unsigned short u16;
typedef unsigned long long u64;
typedef short bf16x8 __attribute__((ext_vector_type(8)));
typedef short v4i16_t __attribute__((ext_vector_type(4)));
typedef __bf16 bf2_t __attribute__((ext_vector_type(2)));
typedef float f32x16 __attribute__((ext_vector_type(16)));
typedef float f32x4 __attribute__((ext_vector_type(4)));
typedef float f32x2 __attribute__((ext_vector_type(2)));
typedef unsigned u32x4 __attribute__((ext_vector_type(4)));
typedef unsigned u32x2 __attribute__((ext_vector_type(2)));
typedef __attribute__((address_space(3))) char* lds_ptr;
typedef __amdgpu_buffer_rsrc_t rsrc_t;
#define DEV __device__ __forceinline__
#define LDS_AS __attribute__((address_space(3)))
#define SBAR() __builtin_amdgcn_sched_barrier(0)
#define PIN(x) asm volatile("" : "+v"(x))
#define MFMA32(a, b, c) __builtin_amdgcn_mfma_f32_32x32x16_bf16(a, b, c, 0, 0, 0)
#define MFMA16(a, b, c) __builtin_amdgcn_mfma_f32_16x16x32_bf16(a, b, c, 0, 0, 0)
#define BAR() asm volatile("s_barrier" ::: "memory")
#define LGKM0() __builtin_amdgcn_s_waitcnt(0xC07F)
#define WAIT_BAR(N) do { LGKM0(); asm volatile("s_waitcnt vmcnt(" #N ")\n\ts_barrier" ::: "memory"); } while (0)
#define LG_BAR() do { asm volatile("" ::: "memory"); LGKM0(); asm volatile("s_barrier" ::: "memory"); } while (0)
#define DRAIN() asm volatile("s_waitcnt vmcnt(0)" ::: "memory")
#define BC8(x) __builtin_bit_cast(bf16x8, x)

template <int N> struct IC { static constexpr int value = N; };
constexpr int S_ = 2048, D_ = 1024, F_ = 3584;
constexpr unsigned KB = 1024, MB = 1u << 20;
constexpr unsigned WS_EPOCH = 0;
constexpr unsigned WS_F1 = 4 * KB;
constexpr unsigned WS_FH = 12 * KB;
constexpr unsigned WS_F2 = 24 * KB;
constexpr unsigned WS_FO = 40 * KB;
constexpr unsigned WS_F3 = 56 * KB;
constexpr unsigned WS_FX = 72 * KB;
constexpr unsigned WS_RS = 128 * KB;
constexpr unsigned WS_G = 160 * KB;
constexpr unsigned WS_ST = 256 * KB;
constexpr unsigned WS_KV = 5 * MB;
constexpr unsigned WS_HS = 13 * MB;
constexpr unsigned WS_X1 = 17 * MB;
constexpr unsigned WS_A4 = 21 * MB;
constexpr unsigned WS_EX = 40 * MB;
constexpr int LDS_BYTES = 160 * 1024;
constexpr unsigned SPIN_CAP = 1u << 18;

#define STAMP(k)
#define STAMP_INIT()
#define STAMP_OUT()

DEV unsigned cvtpk(float lo, float hi) {
    const f32x2 v = {lo, hi};
    return __builtin_bit_cast(unsigned, __builtin_convertvector(v, bf2_t));
}
DEV float bflo(unsigned u) { return __uint_as_float(u << 16); }
DEV float bfhi(unsigned u) { return __uint_as_float(u & 0xffff0000u); }
DEV float rbf(float f) { return bflo(cvtpk(f, f)); }
DEV float dot2(unsigned a, unsigned b, float c) {
    return __builtin_amdgcn_fdot2_f32_bf16(__builtin_bit_cast(bf2_t, a), __builtin_bit_cast(bf2_t, b), c, false);
}
DEV u32x4 pack44(const f32x4& a, const f32x4& b) {
    return (u32x4){cvtpk(a[0], a[1]), cvtpk(a[2], a[3]), cvtpk(b[0], b[1]), cvtpk(b[2], b[3])};
}
DEV float sum32(float v) {
    auto r = __builtin_amdgcn_permlane32_swap(__float_as_uint(v), __float_as_uint(v), false, false);
    return __uint_as_float(r[0]) + __uint_as_float(r[1]);
}
DEV float sum16(float v) {
    auto r = __builtin_amdgcn_permlane16_swap(__float_as_uint(v), __float_as_uint(v), false, false);
    return __uint_as_float(r[0]) + __uint_as_float(r[1]);
}
DEV float max32(float v) {
    auto r = __builtin_amdgcn_permlane32_swap(__float_as_uint(v), __float_as_uint(v), false, false);
    return __builtin_fmaxf(__uint_as_float(r[0]), __uint_as_float(r[1]));
}
template <int CTRL>
DEV float dpp_add(float v) {
    return v + __uint_as_float(__builtin_amdgcn_update_dpp(0u, __float_as_uint(v), CTRL, 0xf, 0xf, true));
}
DEV float wave_sum(float v) {
    v = dpp_add<0xB1>(v);
    v = dpp_add<0x4E>(v);
    v = dpp_add<0x141>(v);
    v = dpp_add<0x140>(v);
    return sum32(sum16(v));
}
DEV bf16x8 ld128(lds_ptr p) { return *(const LDS_AS bf16x8*)p; }
template <int H2 = 2048>
DEV bf16x8 ldtr(lds_ptr p) {
    v4i16_t a = __builtin_amdgcn_ds_read_tr16_b64_v4i16((LDS_AS v4i16_t*)p);
    v4i16_t b = __builtin_amdgcn_ds_read_tr16_b64_v4i16((LDS_AS v4i16_t*)(p + H2));
    return (bf16x8){a[0], a[1], a[2], a[3], b[0], b[1], b[2], b[3]};
}
DEV int rfl(int v) { return __builtin_amdgcn_readfirstlane(v); }
DEV int fresh_lane() {
    int l;
    asm volatile("v_mbcnt_lo_u32_b32 %0, -1, 0\n\tv_mbcnt_hi_u32_b32 %0, -1, %0" : "=v"(l));
    __builtin_assume(l >= 0 && l < 64);
    return l;
}
DEV u32x4 ldw4(rsrc_t r, unsigned v, unsigned s) { return __builtin_amdgcn_raw_buffer_load_b128(r, v, s, 16); }
DEV u32x2 ldw2(rsrc_t r, unsigned v, unsigned s) { return __builtin_amdgcn_raw_buffer_load_b64(r, v, s, 16); }
DEV void stw4(rsrc_t r, unsigned v, unsigned s, u32x4 x) {
    __builtin_amdgcn_raw_buffer_store_b128(x, r, v, s, 16);
    asm volatile("s_nop 1" ::"v"(x) : "memory");
}
DEV void stw2(rsrc_t r, unsigned v, unsigned s, u32x2 x) { __builtin_amdgcn_raw_buffer_store_b64(x, r, v, s, 16); }
constexpr unsigned NOWHERE = 0x80000000u;
#define GLDS(dst, voff, sptr, IMM, SC)                                                                        \
    asm volatile("s_mov_b32 m0, %0\n\ts_nop 0\n\tglobal_load_lds_dwordx4 %1, %2 offset:" #IMM SC ::"s"(dst), "v"(voff), \
                 "s"(sptr) : "memory")
#define IF_EQ(t, K) "s_cmp_lg_u32 " t ", " #K "\n\ts_cbranch_scc1 .Lskip%=\n\t"
#define END_IF "\n.Lskip%=:"
DEV void post_flag(rsrc_t r, unsigned off, u32x2 tag, int lane) {
    DRAIN();
    stw2(r, lane == 0 ? 0 : NOWHERE, off, tag);
}
DEV bool any(unsigned v) { return __builtin_amdgcn_ballot_w64(v != 0) != 0; }
DEV unsigned diff(u32x2 v, u32x2 tag) { return (v[0] ^ tag[0]) | (v[1] ^ tag[1]); }
DEV void wait_flags(rsrc_t r, unsigned voff, u32x2 tag) {
#pragma unroll 1
    for (unsigned n = 0; n < SPIN_CAP; ++n) {
        asm volatile("" ::: "memory");
        const u32x2 v = ldw2(r, voff, 0);
        if (!any(diff(v, tag))) break;
        __builtin_amdgcn_s_sleep(2);
    }
    asm volatile("" ::: "memory");
}
#define RETRY(BAD, LOADS)                                                    \
    if (__builtin_expect(any(BAD), 0)) {                                     \
        _Pragma("unroll 1") for (unsigned n_ = 0; n_ < SPIN_CAP; ++n_) {     \
            __builtin_amdgcn_s_sleep(2);                                     \
            asm volatile("" ::: "memory");                                   \
            LOADS;                                                           \
            __builtin_amdgcn_s_waitcnt(0x0F70);                              \
            if (!any(BAD)) break;                                            \
        }                                                                    \
    }
DEV f32x2 gelu2(f32x2 v, float a, float b, float k) {
    const f32x2 y = v * __builtin_elementwise_fma(v * v, (f32x2){b, b}, (f32x2){a, a});
    const f32x2 e = {__builtin_amdgcn_exp2f(y[0]), __builtin_amdgcn_exp2f(y[1])};
    const f32x2 d = __builtin_elementwise_fma(e, (f32x2){k, k}, (f32x2){k, k});
    return v * (f32x2){__builtin_amdgcn_rcpf(d[0]), __builtin_amdgcn_rcpf(d[1])};
}
constexpr float GC0 = -2.0f * 0.7978845608028654f * 1.4426950408889634f, GC1 = GC0 * 0.044715f;
DEV u32x4 ln_gelu_chunk(const f32x4& p, const f32x4& q, const f32x4& cp, const f32x4& cq, float mean, float a, float b, float k) {
    const f32x2 nm = {-mean, -mean};
    const f32x2 r0 = gelu2(__builtin_elementwise_fma(nm, (f32x2){cp[0], cp[1]}, (f32x2){p[0], p[1]}), a, b, k);
    const f32x2 r1 = gelu2(__builtin_elementwise_fma(nm, (f32x2){cp[2], cp[3]}, (f32x2){p[2], p[3]}), a, b, k);
    const f32x2 r2 = gelu2(__builtin_elementwise_fma(nm, (f32x2){cq[0], cq[1]}, (f32x2){q[0], q[1]}), a, b, k);
    const f32x2 r3 = gelu2(__builtin_elementwise_fma(nm, (f32x2){cq[2], cq[3]}, (f32x2){q[2], q[3]}), a, b, k);
    return (u32x4){cvtpk(r0[0], r0[1]), cvtpk(r1[0], r1[1]), cvtpk(r2[0], r2[1]), cvtpk(r3[0], r3[1])};
}

constexpr unsigned STG_A = 49152, CSL = 3 * STG_A;
constexpr unsigned RCL = 150 * 1024;
struct LoadA {
    const char *a, *a3, *b;
    unsigned va[4], vb[2], da;
    u64 ta;
    unsigned tstep, scrap;
    unsigned post;
    lds_ptr xb[2], wb[4], wb1[4];
};
template <bool FO>
DEV void setupA(LoadA& L, int lane, int wid, unsigned lds0, const char* xs, const char* w0, const char* w1) {
    const int ir = 8 * wid + (lane >> 3);
    const unsigned wch = ((lane & 7) ^ (((ir >> 1) & 3) << 1)) << 4;
    if (FO) {
        L.a = L.a3 = xs; L.b = w0 - 896;
        L.va[0] = L.va[1] = (2 * wid) * 32768 + lane * 16;
        L.va[2] = L.va[3] = L.va[0] + 32768 - 2048;
        L.vb[1] = ir * (F_ * 2) + wch;
        L.vb[0] = L.vb[1] + 896;
    } else {
        const int sr = (ir & 32) | ((ir & 12) << 1) | ((ir & 16) >> 2) | (ir & 3);
        L.a = w0; L.a3 = w1 - 3072; L.b = xs;
        L.va[0] = sr * 6144 + wch; L.va[1] = L.va[0] + 1024; L.va[2] = L.va[0] + 2048; L.va[3] = sr * 2048 + wch;
#pragma unroll
        for (int i = 0; i < 2; ++i) {
            const int row = 16 * wid + 8 * i + (lane >> 3);
            L.vb[i] = row * 2048 + (((lane & 7) ^ ((row >> 1) & 7)) << 4) - i * 1024;
        }
    }
    L.da = lds0 + wid * 4096;
}
template <bool FO>
DEV void setupR(LoadA& L, char* smem, int lane, int wid) {
    const int c = lane & 15, g = lane >> 4, q = c >> 2, grp = wid >> 2;
    const int sx = (FO || grp) ? (wid & 1) : 0, sw = (2 * g + (q >> 1)) & 3;
    const lds_ptr wr = (lds_ptr)smem + ((4 * g + q) & 7) * 128 + (lane & 3) * 8;
#pragma unroll
    for (int s = 0; s < 2; ++s) {
        const int sa = s ^ sx;
        L.xb[s] = (lds_ptr)smem + (FO ? (8 * (wid & 3) + sa) * 1024 + lane * 16
                                      : 32768 + (64 * (wid & 1) + c) * 128 + (((4 * sa + g) ^ ((c >> 1) & 7)) << 4));
        PIN(L.xb[s]);
    }
#pragma unroll
    for (int nb = 0; nb < (FO ? 4 : 2); ++nb) {
        L.wb[nb] = wr + (((nb ^ (wid & 2)) ^ sw) << 5) +
                   (FO ? 32768 + grp * 1024 + (g >> 1) * 2048 + sx * 8192 : (1 - grp) * 1024 + (g >> 1) * 4096 + sx * 16384);
        L.wb1[nb] = L.wb[nb] + (FO ? 8192 : 16384) * (1 - 2 * sx);
        PIN(L.wb[nb]); PIN(L.wb1[nb]);
    }
}
#define M0SET(v) asm volatile("s_mov_b32 m0, %0\n\ts_nop 0" ::"s"(v) : "memory")
#define GL(voff, sptr, IMM, SC) asm volatile("global_load_lds_dwordx4 %0, %1 offset:" #IMM SC ::"v"(voff), "s"(sptr) : "memory")
#define A_SETA(L, so) M0SET(L.da + (so))
#define A_SETB(L, so, wid) M0SET(L.da + 32768 - (wid) * 2048 + (so))
#define A_A(L, FO, i, IMM) do { if (FO) GL(L.va[i], L.a, IMM, " sc1"); else if (i < 3) GL(L.va[i], L.a, IMM, ""); else GL(L.va[i], L.a3, IMM, ""); } while (0)
#define A_ALLA(L, FO, so) do { A_SETA(L, so); A_A(L, FO, 0, 0); A_A(L, FO, 1, 1024); A_A(L, FO, 2, 2048); A_A(L, FO, 3, 3072); A_NEXTA(L, FO); } while (0)
#define A_ALLB(L, FO, so, wid) do { A_SETB(L, so, wid); GL(L.vb[0], L.b, 0, ""); GL(L.vb[1], L.b, 1024, ""); A_NEXTB(L, FO); } while (0)
#define A_NEXTA(L, FO) do { L.a += FO ? 2048 : 64 * 6144; if (!FO) L.a3 += 64 * 2048; } while (0)
#define A_NEXTB(L, FO) L.b += FO ? 64 * F_ * 2 : 128
#define A_TOUCH(L) asm volatile("global_load_dword %0, %1, off" : "+v"(L.scrap) : "v"(L.ta) : "memory")
#define POST_BAR(N, N0, t, off, tag, ws)                                                                       \
    do {                                                                                                       \
        LGKM0();                                                                                               \
        asm volatile("s_cmp_eq_u32 %0, 0\n\ts_waitcnt vmcnt(" #N0 ")\n\ts_cbranch_scc1 .Lskip%=\n\t"          \
                     "s_cmp_lg_u32 %0, 1\n\ts_waitcnt vmcnt(" #N ")\n\ts_cbranch_scc1 .Lskip%=\n\t"           \
                     "s_mov_b64 exec, 1\n\tglobal_store_dwordx2 %1, %2, %3 sc1\n\ts_mov_b64 exec, -1"         \
                     "\n.Lskip%=:\n\ts_add_u32 %0, %0, 1\n\ts_barrier"                                        \
                     : "+s"(t) : "v"(off), "v"(tag), "s"(ws) : "memory", "scc");                               \
    } while (0)

template <int mode, class Hook, class Hook2>
DEV void kloopA(char* ws, u32x2 tag, char* smem, LoadA& L, int lane, int wid, f32x4 (&acc)[4][4], Hook hook, Hook2 hook2) {
    constexpr bool FO = mode == 2;
    constexpr int H2 = FO ? 4096 : 8192, NP = FO ? 4 : 2;
    float cs0 = 0.f, cs1 = 0.f, cs2 = 0.f, cs3 = 0.f;
    unsigned so2 = 2 * STG_A, tm = ~0u;
    int tc = FO ? 0 : -11;
    if (FO) { PIN(tag); PIN(L.post); }
    auto body = [&](auto kind) {
        constexpr int KIND = decltype(kind)::value;
        bf16x8 xf[4][2], wf[4][2];
#define RD(i, s)                                                                                                     \
    wf[i][s] = ldtr<H2>((s && mode ? L.wb1[i % NP] : L.wb[i % NP]) + (s && !mode ? 16384 : 0) + (i / NP) * (mode ? 3072 : 1024)); \
    xf[i][s] = ld128(L.xb[s] + i * 2048); SBAR()
        SBAR();
        if (KIND == 1) { hook(); SBAR(); }
        if (KIND == 2) { hook2(); SBAR(); }
        RD(0, 0);
        if (KIND == 0) { A_SETA(L, so2); A_A(L, FO, 0, 0); SBAR(); }
        RD(1, 0);
        if (KIND == 0) { A_A(L, FO, 1, 1024); SBAR(); }
        RD(2, 0);
        if (KIND == 0) { A_A(L, FO, 2, 2048); SBAR(); }
        RD(3, 0);
        if (KIND == 0) { A_A(L, FO, 3, 3072); SBAR(); }
        RD(0, 1);
        if (KIND == 0) { A_SETB(L, so2, wid); GL(L.vb[0], L.b, 0, ""); SBAR(); }
        RD(1, 1);
        if (KIND == 0) { GL(L.vb[1], L.b, 1024, ""); SBAR(); }
        RD(2, 1);
        if (KIND == 0 && !FO) { A_TOUCH(L); SBAR(); }
        RD(3, 1);
#undef RD
        int adv = so2 == STG_A ? -2 * (int)STG_A : (int)STG_A;
        so2 = so2 == 2 * STG_A ? 0 : so2 + STG_A;
        if (KIND == 0) { A_NEXTA(L, FO); A_NEXTB(L, FO); }
        asm volatile("" : "+s"(adv), "+s"(so2), "+s"(L.a), "+s"(L.b));
        if (!FO && KIND == 0) asm volatile("s_ashr_i32 %0, %1, 31\n\ts_add_i32 %1, %1, 1" : "=&s"(tm), "+s"(tc), "+s"(L.a3)::"scc");
        SBAR();
        if (KIND == 0) { if (FO) POST_BAR(6, 8, tc, L.post, tag, ws); else WAIT_BAR(8); } else if (KIND == 1) { __builtin_amdgcn_s_waitcnt(0x0070); BAR(); } else LG_BAR();
        if (KIND == 1 && !FO) PIN(L.scrap);
        SBAR();
#define ADV(i)                                                                                       \
    do {                                                                                             \
        if (KIND != 2) {                                                                             \
            if ((i) >= 18 && (i) < 20) { L.xb[(i) & 1] += adv; PIN(L.xb[(i) & 1]); }                 \
            if ((i) >= 20 && (i) < 20 + NP) { L.wb[(i) & 3] += adv; PIN(L.wb[(i) & 3]); }            \
            if ((i) >= 24 && (i) < 24 + NP && mode) { L.wb1[(i) & 3] += adv; PIN(L.wb1[(i) & 3]); }  \
        }                                                                                            \
        if (KIND == 0 && !FO) {                                                                      \
            if ((i) == 22) { L.tstep &= tm; PIN(L.tstep); }                                          \
            if ((i) == 23) { L.ta += L.tstep; PIN(L.ta); }                                           \
        }                                                                                            \
        SBAR();                                                                                      \
    } while (0)
        if (mode == 0) {
#pragma unroll
            for (int i = 0; i < 32; ++i) {
                if (i & 2) acc[i & 3][(i >> 2) & 3] = MFMA16(xf[(i >> 2) & 3][i >> 4], wf[i & 3][i >> 4], acc[i & 3][(i >> 2) & 3]);
                else acc[i & 3][(i >> 2) & 3] = MFMA16(wf[i & 3][i >> 4], xf[(i >> 2) & 3][i >> 4], acc[i & 3][(i >> 2) & 3]);
                ADV(i);
            }
        } else {
            const u32x4 u0 = __builtin_bit_cast(u32x4, wf[mode == 1 ? 2 : 0][0]), u1 = __builtin_bit_cast(u32x4, wf[mode == 1 ? 3 : 1][0]);
            float tmp = 0.f;
#pragma unroll
            for (int i = 0; i < 32; ++i) {
                const unsigned u = (i & 16) ? u1[(i >> 2) & 3] : u0[(i >> 2) & 3];
                if (i == 31) { cs3 += tmp; PIN(cs3); SBAR(); }
                acc[i & 3][(i >> 2) & 3] = MFMA16(wf[i & 3][i >> 4], xf[(i >> 2) & 3][i >> 4], acc[i & 3][(i >> 2) & 3]);
                if ((i & 1) == 0) { tmp = (i & 2) ? bfhi(u) : bflo(u); PIN(tmp); }
                else if (i == 31) {}
                else if (i & 16) { if (i & 2) { cs3 += tmp; PIN(cs3); } else { cs2 += tmp; PIN(cs2); } }
                else { if (i & 2) { cs1 += tmp; PIN(cs1); } else { cs0 += tmp; PIN(cs0); } }
                ADV(i);
            }
        }
#undef ADV
        if (KIND != 2) BAR();
        SBAR();
    };
#pragma unroll 1
    for (int t = 0; t < 14; ++t) body(IC<0>{});
    body(IC<1>{});
    body(IC<2>{});
    if (mode) {
        const float a = sum32(sum16(cs0 + cs1)), b = sum32(sum16(cs2 + cs3));
        LDS_AS float* csl = (LDS_AS float*)((lds_ptr)smem + CSL) + (wid >> 2) * 128 + (wid & 1) * 64 + (wid & 2) * 16 + (lane & 15);
        csl[0] = a; csl[16] = b;
    }
    if (wid < 4) LG_BAR();
}
template <int N>
DEV void read_cs(char* smem, int lane, int wid, f32x4 (&cs)[N]) {
    const LDS_AS float* csl = (const LDS_AS float*)((lds_ptr)smem + CSL) + (wid >> 2) * 128 + (lane >> 4) * 4;
#pragma unroll
    for (int nb = 0; nb < N; ++nb)
        cs[nb] = *(const LDS_AS f32x4*)(csl + (nb ^ (wid & 2)) * 16) + *(const LDS_AS f32x4*)(csl + 64 + (nb ^ (wid & 2)) * 16);
}

__global__ __launch_bounds__(512, 2) void k_mega(const u16* __restrict__ x, const u16* __restrict__ wqkv,
                                                 const u16* __restrict__ wo, const u16* __restrict__ w1,
                                                 const u16* __restrict__ w2, const u16* __restrict__ w3,
                                                 const u16* __restrict__ w4, const u16* __restrict__ gate_w,
                                                 char* __restrict__ ws, u16* __restrict__ out) {
    extern __shared__ __attribute__((aligned(16))) char smem[];
    const int b = blockIdx.x, xcd = b & 7, jb = b >> 3;
    const int wid = rfl(threadIdx.x >> 6);
    const unsigned lds0 = (unsigned)(uintptr_t)(lds_ptr)smem;
    const rsrc_t R = __builtin_amdgcn_make_buffer_rsrc(ws, 0, 0x7fffffff, 0x00020000);
    const int hp = 2 * xcd + (jb >> 4), rt = jb & 15;
    const int head = 2 * hp + (wid & 1), ks = wid >> 1, grp = wid >> 2;
    STAMP_INIT();
    STAMP(0);
    u32x2 tag;
    bf16x8 Q[4][2], Ka[2], Kb[2], Va[2], Vb[2], K0[4];
    unsigned fo1;
    auto phase1 = [&](auto gc) {
        constexpr int G = decltype(gc)::value;
        const int lane = fresh_lane(), c = lane & 15, g = lane >> 4, wm = wid & 1, hh = (wid >> 1) & 1, h1 = 2 * hp + hh;
        LoadA L;
        setupA<false>(L, lane, wid, lds0, (const char*)(x + (size_t)rt * 128 * D_), (const char*)(wqkv + 64 * hp),
                      (const char*)(w1 + 64 * hp));
        u32x2 ep;
        {
            const unsigned z = 0;
            asm volatile("global_load_dwordx2 %0, %1, %2 sc1" : "=v"(ep) : "v"(z), "s"(ws) : "memory");
        }
        A_ALLB(L, false, 0, wid); A_ALLA(L, false, 0);
        const int srow = rt * 128 + hp * 8 + (wid & 3) * 2;
        const char* xr = (const char*)(x + (size_t)srow * D_);
        u32x4 gx[4];
        if (G)
            asm volatile("global_load_dwordx4 %0, %4, %5\n\tglobal_load_dwordx4 %1, %4, %5 offset:1024\n\t"
                         "global_load_dwordx4 %2, %4, %5 offset:2048\n\tglobal_load_dwordx4 %3, %4, %5 offset:3072"
                         : "=&v"(gx[0]), "=&v"(gx[1]), "=&v"(gx[2]), "=&v"(gx[3]) : "v"(lane * 16), "s"(xr) : "memory");
        A_ALLB(L, false, STG_A, wid); A_ALLA(L, false, STG_A);
        {
            const int j = (jb * 8 + wid) * 4 + (lane >> 4), m = (j >> 1) & 3;
            const unsigned col = (2 * xcd + (j & 1)) * 128;
            const bool w = jb < 16, w3rd = m < 3;
            L.tstep = !w ? 128 : w3rd ? 64 * 6144 : 64 * 2048;
            L.ta = (!w ? (u64)x : w3rd ? (u64)wqkv : (u64)w1) +
                   (!w ? ((xcd & 3) * 512 + j - 512) * 2048 + 384 : (192 + (j >> 3)) * (L.tstep >> 6) + (w3rd ? m * 2048 : 0) + col);
            L.scrap = 0;
            A_TOUCH(L);
            L.ta += L.tstep;
        }
        setupR<false>(L, smem, lane, wid);
        f32x4 acc[4][4] = {};
#pragma unroll
        for (int i = 0; i < 16; ++i) PIN(acc[i >> 2][i & 3]);
        if (G) asm volatile("s_waitcnt vmcnt(11)\n\ts_barrier\n\ts_waitcnt vmcnt(7)" : "+v"(ep), "+v"(gx[0]), "+v"(gx[1]), "+v"(gx[2]), "+v"(gx[3])::"memory");
        else asm volatile("s_waitcnt vmcnt(7)\n\ts_barrier" : "+v"(ep)::"memory");
        {
            const u64 e = ((u64)(unsigned)rfl(ep[1]) << 32 | (unsigned)rfl(ep[0])) + 1;
            tag = (u32x2){(unsigned)e, (unsigned)(e >> 32)};
        }
        STAMP(1);
        const int rg = rt * 2 + wm;
        constexpr unsigned XQ = 32768;
        u32x4 rsv[4];
        if (G) {
            const unsigned rso = WS_RS + (rg * 64 + c) * 16;
            float s1[2] = {0.f, 0.f}, s2[2] = {0.f, 0.f};
#pragma unroll
            for (int i = 0; i < 4; ++i)
#pragma unroll
                for (int e = 0; e < 4; ++e) {
                    const unsigned u = gx[i][e];
                    s1[i >> 1] = dot2(u, 0x3F803F80u, s1[i >> 1]);
                    s2[i >> 1] = dot2(u, u, s2[i >> 1]);
                }
            auto p1 = __builtin_amdgcn_permlane32_swap(__float_as_uint(s1[0]), __float_as_uint(s1[1]), false, false);
            auto p2 = __builtin_amdgcn_permlane32_swap(__float_as_uint(s2[0]), __float_as_uint(s2[1]), false, false);
            float a1 = __uint_as_float(p1[0]) + __uint_as_float(p1[1]), a2 = __uint_as_float(p2[0]) + __uint_as_float(p2[1]);
            a1 = dpp_add<0xB1>(a1); a2 = dpp_add<0xB1>(a2); a1 = dpp_add<0x4E>(a1); a2 = dpp_add<0x4E>(a2);
            a1 = dpp_add<0x141>(a1); a2 = dpp_add<0x141>(a2); a1 = dpp_add<0x140>(a1); a2 = dpp_add<0x140>(a2);
            a1 = sum16(a1); a2 = sum16(a2);
            const float mean = a1 * (1.0f / D_);
            const float rstd = __builtin_amdgcn_rsqf(__builtin_fmaxf(a2 * (1.0f / D_) - mean * mean, 0.f) + 1e-5f);
            stw2(R, (lane & 30) ? NOWHERE : (lane >> 5) * 16 + (lane & 1) * 8, WS_RS + srow * 16,
                 (u32x2){__float_as_uint((lane & 1) ? rstd : mean), tag[0]});
            BAR();
            kloopA<1>(ws, tag, smem, L, lane, wid, acc, [&]() {
#pragma unroll
                for (int mb = 0; mb < 4; ++mb) rsv[mb] = ldw4(R, rso + mb * 256, 0);
            }, [&]() {
                const u16* w2p = w2;
                M0SET(lds0 + 148 * 1024);
                asm volatile("global_load_lds_dword %0, %1" ::"v"((((jb & 3) * 4 + (wid & 3)) * 64 + lane) * 2048 + (8 * (xcd & 1) + (jb >> 2)) * 128),
                             "s"(w2p) : "memory");
            });
            STAMP(2);
            const float sc = 0.17677669529663687f * 1.4426950408889634f;
            LDS_AS u32x4* qd = (LDS_AS u32x4*)((lds_ptr)smem + XQ + (wid & 3) * 4096 + (g >> 1) * 1024) + (g & 1) * 32 + c;
#pragma unroll
            for (int mb = 0; mb < 4; ++mb) qd[(mb >> 1) * 128 + (mb & 1) * 16] = pack44(acc[0][mb] * sc, acc[1][mb] * sc);
            RETRY(((rsv[0][1] & rsv[0][3] & rsv[1][1] & rsv[1][3] & rsv[2][1] & rsv[2][3] & rsv[3][1] & rsv[3][3]) ^ tag[0]) |
                      ((rsv[0][1] | rsv[0][3] | rsv[1][1] | rsv[1][3] | rsv[2][1] | rsv[2][3] | rsv[3][1] | rsv[3][3]) ^ tag[0]),
                  for (int mb = 0; mb < 4; ++mb) rsv[mb] = ldw4(R, rso + mb * 256, 0));
            fo1 = WS_FH + h1 * 8 + (lane == 0 ? (rg + 1) * 256 : rg == 0 ? 33 * 256 : 0);
            if (lane > (rg == 0 || rg == 31)) fo1 = NOWHERE;
        } else {
            u32x4 gw[2];
            kloopA<0>(ws, tag, smem, L, lane, wid, acc, [&]() {
#pragma unroll
                for (int i = 0; i < 4; ++i) gx[i] = *(const u32x4*)(xr + i * 1024 + lane * 16);
                gw[0] = *(const u32x4*)(gate_w + lane * 8);
                gw[1] = *(const u32x4*)(gate_w + 512 + lane * 8);
            }, []() {});
            STAMP(2);
            const unsigned ko = (g >> 1) * 1024 + ((g & 1) * 32 + (c >> 3) * 16 + (c & 7)) * 16;
            const unsigned vo = 2048 + (g >> 1) * 1024 + ((g & 1) * 32 + c) * 16;
            const lds_ptr kd = (lds_ptr)smem + wid * 8192;
            const unsigned kvg = WS_KV + (h1 * 64 + 2 * rg) * 4096;
#pragma unroll
            for (int i = 0; i < 4; ++i) {
                const u32x4 kk = pack44(acc[0][i], acc[1][i]);
                const u32x4 vv = pack44(acc[2 + (i & 1)][i & 2], acc[2 + (i & 1)][(i & 2) + 1]);
                stw4(R, ko + (i & 1) * 128, kvg + (i >> 1) * 4096, kk);
                stw4(R, vo + (i & 1) * 256, kvg + (i >> 1) * 4096, vv);
                *(LDS_AS u32x4*)(kd + ko + (i >> 1) * 4096 + (i & 1) * 128) = kk;
                *(LDS_AS u32x4*)(kd + vo + (i >> 1) * 4096 + (i & 1) * 256) = vv;
            }
            SBAR();
            float z[2] = {0.f, 0.f};
#pragma unroll
            for (int i = 0; i < 4; ++i)
#pragma unroll
                for (int e = 0; e < 4; ++e) {
                    const unsigned u = gx[i][e], w = gw[i & 1][e];
                    z[i >> 1] = dot2(cvtpk(bflo(u) * bflo(w), bfhi(u) * bfhi(w)), 0x3F803F80u, z[i >> 1]);
                }
            auto p1 = __builtin_amdgcn_permlane32_swap(__float_as_uint(z[0]), __float_as_uint(z[1]), false, false);
            float a1 = __uint_as_float(p1[0]) + __uint_as_float(p1[1]);
            a1 = dpp_add<0xB1>(a1); a1 = dpp_add<0x4E>(a1); a1 = dpp_add<0x141>(a1); a1 = dpp_add<0x140>(a1);
            a1 = rbf(sum16(a1));
            const float gt = rbf(1.0f / (1.0f + __expf(-a1)));
            stw2(R, (lane & 31) ? NOWHERE : (lane >> 5) * 8, WS_G + srow * 8, (u32x2){cvtpk(gt, 1.0f - gt), tag[0]});
            STAMP(22);
            post_flag(R, WS_F1 + (h1 * 32 + rg) * 8, tag, lane);
            fo1 = NOWHERE;
        }
        STAMP(19);
        LG_BAR();
        {
            const lds_ptr kd = (lds_ptr)smem + (wid & 1) * 16384 + lane * 16, qd = (lds_ptr)smem + XQ + (wid & 1) * 8192 + lane * 16;
#pragma unroll
            for (int f = 0; f < 2; ++f) {
                K0[f] = ld128(kd + f * 1024); K0[2 + f] = ld128(qd + f * 1024);
#pragma unroll
                for (int i = 0; i < 4; ++i) Q[i][f] = ld128(qd + (i ^ ks) * 2048 + f * 1024);
                Ka[f] = ld128(kd + ks * 4096 + f * 1024); Va[f] = ld128(kd + ks * 4096 + 2048 + f * 1024);
            }
        }
        f32x4 cs[2];
        if (G) read_cs(smem, lane, wid, cs);
        LG_BAR();
        STAMP(3);
        if (G) {
            const int quarter = hp >> 2;
            const int sh = quarter == 0 ? 1 : quarter == 1 ? -1 : quarter == 2 ? 2 : -2;
#pragma unroll
            for (int mb = 0; mb < 4; ++mb) {
                const float mean = __uint_as_float(rsv[mb][0]), r = __uint_as_float(rsv[mb][2]);
                const float a = GC0 * r, bq = GC1 * r * r * r, k = __builtin_amdgcn_rcpf(r);
                const int t = rg * 64 + mb * 16 + c + sh, tw = t & (S_ - 1);
                u32x4 v = ln_gelu_chunk(acc[2][mb], acc[3][mb], cs[0], cs[1], mean, a, bq, k);
                if (mb == 0 || mb == 3) v &= t == tw ? ~0u : 0u;
                stw4(R, (tw >> 4) * 32768 + (g * 16 + (tw & 15)) * 16, WS_HS + h1 * 1024, v);
            }
        }
        STAMP(4);
    };
    if (grp) phase1(IC<1>{}); else phase1(IC<0>{});
    const bool p3_on = jb < 28;
    const int mt3 = 4 * (xcd & 1) + (jb & 3), nt3 = 7 * (xcd >> 1) + (jb >> 2);
    LoadA L3;
    {
        int lane = fresh_lane();
        const unsigned kvh = WS_KV + head * 64 * 4096;
        unsigned vl = lane * 16;
        constexpr unsigned PART = 128 * 1024;
        int kt = rt >> 1;
#define KVO(t) (kvh + ((t) * 8 + 2 * ks) * 4096)
#define LDF(so, imm) BC8(ldw4(R, vl + (imm), so))
        float mref = -INFINITY;
        {
            f32x16 s = MFMA32(K0[0], K0[2], (f32x16){});
            s = MFMA32(K0[1], K0[3], s);
#pragma unroll
            for (int r = 0; r < 16; r += 2) mref = __builtin_fmaxf(__builtin_fmaxf(s[r], s[r + 1]), mref);
        }
        mref = max32(mref);
        f32x16 O[4] = {};
        f32x4 Ls = {};
#define EX(d, X, i) asm volatile("v_exp_f32 %0, %1" : "=v"(d) : "v"(X[i]))
#define CV(P, k, a, b) do { unsigned t_; asm volatile("v_cvt_pk_bf16_f32 %0, %1, %2" : "=v"(t_) : "v"(a), "v"(b)); P[(k) >> 2][(k) & 3] = t_; } while (0)
#define MM32(D, A, B) do { SBAR(); D = MFMA32(BC8(A), BC8(B), D); PIN(D); SBAR(); } while (0)
#define MM16(D, A, B) asm volatile("v_mfma_f32_16x16x32_bf16 %0, %1, %2, %0" : "+v"(D) : "v"(A), "v"(B))
#define STEP(Sc, Sn, Pc, Pp, Kn, qn, Vp, bp) STEPX(true, Sc, Sn, Pc, Pp, Kn, qn, Vp, bp)
#define STEPX(NEXT, Sc, Sn, Pc, Pp, Kn, qn, Vp, bp)                                                             \
    do {                                                                                                        \
        float a0, a1, a2, a3, b0, b1, b2, b3;                                                                   \
        SBAR();                                                                                                 \
        if (NEXT) { Sn = MFMA32(Kn[0], Q[qn][0], C); PIN(Sn); SBAR(); }                                         \
        EX(a0, Sc, 0); EX(a1, Sc, 1); EX(a2, Sc, 2); EX(a3, Sc, 3); CV(Pp, 6, E[0], E[1]); CV(Pp, 7, E[2], E[3]); \
        MM32(O[bp], Vp[0], Pp[0]);                                                                              \
        EX(b0, Sc, 4); EX(b1, Sc, 5); EX(b2, Sc, 6);                                                            \
        MM16(Ls, sel[bp], Pp[0]);                                                                               \
        EX(b3, Sc, 7); CV(Pc, 0, a0, a1); CV(Pc, 1, a2, a3);                                                    \
        if (NEXT) MM32(Sn, Kn[1], Q[qn][1]);                                                                    \
        EX(a0, Sc, 8); EX(a1, Sc, 9); EX(a2, Sc, 10); EX(a3, Sc, 11); CV(Pc, 2, b0, b1); CV(Pc, 3, b2, b3);     \
        MM32(O[bp], Vp[1], Pp[1]);                                                                              \
        EX(E[0], Sc, 12); EX(E[1], Sc, 13); EX(E[2], Sc, 14);                                                   \
        MM16(Ls, sel[bp], Pp[1]);                                                                               \
        EX(E[3], Sc, 15); CV(Pc, 4, a0, a1); CV(Pc, 5, a2, a3);                                                 \
        SBAR();                                                                                                 \
    } while (0)
        const char* t3 = (const char*)w3 + (b * 8 + wid) * 512;
        unsigned scrap = 0;
#define TCH3()                                                                                                  \
    do {                                                                                                        \
        asm volatile("s_load_dword %0, %1, 0x0\n\ts_load_dword %0, %1, 0x40\n\ts_load_dword %0, %1, 0x80\n\t"  \
                     "s_load_dword %0, %1, 0xc0\n\ts_load_dword %0, %1, 0x100\n\ts_load_dword %0, %1, 0x140\n\t" \
                     "s_load_dword %0, %1, 0x180\n\ts_load_dword %0, %1, 0x1c0" : "+s"(scrap) : "s"(t3));       \
        t3 += 1 << 20;                                                                                          \
    } while (0)
#define ATT_INIT()                                                                                              \
        bf16x8 sel[4];                                                                                          \
        _Pragma("unroll") for (int i = 0; i < 4; ++i) {                                                         \
            const short v = ((lane & 3) == i && ((lane >> 2) & 1) == ((lane >> 4) & 1)) ? (short)0x3F80 : (short)0; \
            sel[i] = (bf16x8){v, v, v, v, v, v, v, v};                                                          \
            PIN(sel[i]);                                                                                        \
        }                                                                                                       \
        f32x16 C, SA, SB;                                                                                       \
        PIN(mref);                                                                                              \
        _Pragma("unroll") for (int r = 0; r < 16; ++r) C[r] = -mref;                                            \
        PIN(C);                                                                                                 \
        u32x4 PA[2] = {}, PB[2] = {};                                                                           \
        float E[4] = {};                                                                                        \
        SA = MFMA32(Ka[0], Q[0][0], C);                                                                         \
        SA = MFMA32(Ka[1], Q[0][1], SA);                                                                        \
        asm volatile("s_nop 7\n\ts_nop 7" : "+v"(SA), "+v"(PB[0]), "+v"(PB[1]), "+v"(Vb[0]), "+v"(Vb[1]), "+v"(O[0]),     \
                     "+v"(O[1]), "+v"(O[2]), "+v"(O[3]), "+v"(Ls))
#define ATT_FLAG(fl, fa) if (__builtin_expect(any(diff(fl, tag)), 0)) wait_flags(R, fa, tag)
#define ATT_RUN(nit, TOP, MID)                                                                                  \
        _Pragma("unroll 1") for (int it = 0; it < (nit); ++it) {                                                \
            const unsigned kvo = KVO(kt);                                                                       \
            kt = (kt + 1) & 7;                                                                                  \
            const unsigned n2 = KVO(kt), fa = WS_F1 + (head * 32 + kt * 4 + ks) * 8;                            \
            TOP;                                                                                                \
            const u32x2 fl = ldw2(R, 0, fa);                                                                    \
            STEP(SA, SB, PA, PB, Ka, 1, Vb, 3);                                                                 \
            Vb[0] = LDF(kvo + 4096, 2048); Vb[1] = LDF(kvo + 4096, 3072);                                       \
            TCH3();                                                                                             \
            STEP(SB, SA, PB, PA, Ka, 2, Va, 0);                                                                 \
            MID;                                                                                                \
            STEP(SA, SB, PA, PB, Ka, 3, Va, 1);                                                                 \
            ATT_FLAG(fl, fa);                                                                                   \
            Ka[0] = LDF(n2, 0); Ka[1] = LDF(n2, 1024);                                                          \
            STEP(SB, SA, PB, PA, Kb, 0, Va, 2);                                                                 \
            MID;                                                                                                \
            STEP(SA, SB, PA, PB, Kb, 1, Va, 3);                                                                 \
            Va[0] = LDF(n2, 2048); Va[1] = LDF(n2, 3072);                                                       \
            STEP(SB, SA, PB, PA, Kb, 2, Vb, 0);                                                                 \
            MID;                                                                                                \
            STEP(SA, SB, PA, PB, Kb, 3, Vb, 1);                                                                 \
            Kb[0] = LDF(n2 + 4096, 0); Kb[1] = LDF(n2 + 4096, 1024);                                            \
            STEP(SB, SA, PB, PA, Ka, 0, Vb, 2);                                                                 \
            MID;                                                                                                \
        }
#define ATT_END()                                                                                               \
        CV(PB, 6, E[0], E[1]); CV(PB, 7, E[2], E[3]);                                                           \
        MM32(O[3], Vb[0], PB[0]); MM16(Ls, sel[3], PB[0]); MM32(O[3], Vb[1], PB[1]); MM16(Ls, sel[3], PB[1]);   \
        asm volatile("s_nop 7\n\ts_nop 7\n\ts_nop 3\n\ts_waitcnt lgkmcnt(0)"                                     \
                     : "+v"(O[0]), "+v"(O[1]), "+v"(O[2]), "+v"(O[3]), "+v"(Ls), "+s"(scrap))
        const int mt2 = 4 * (xcd >> 1) + (jb & 3), nt2 = 8 * (xcd & 1) + (jb >> 2);
        const int n1 = grp ? 5 : 3;
        {
            const int ir = lane >> 3;
            const unsigned wv = ir * 2048 + (((lane & 7) ^ (((ir >> 1) & 3) << 1)) << 4);
            const char* wp = (const char*)(w2 + (size_t)(128 * wid) * D_ + nt2 * 64);
            unsigned wd = lds0 + wid * 16384;
#define W2L() do { GLDS(wd, wv, wp, 0, ""); wd += 1024; wp += 16384; } while (0)
            STAMP(5);
            Vb[0] = Vb[1] = (bf16x8){};
            ATT_INIT();
            {
                kt = (kt + 1) & 7;
                const unsigned nb = kvh + ((rt ^ 1) * 4 + ks) * 4096, n2 = KVO(kt);
                const unsigned fa = WS_F1 + (head * 32 + (lane < 32 ? (rt ^ 1) * 2 + (ks >> 1) : kt * 4 + ks)) * 8;
                STEP(SA, SB, PA, PB, Ka, 1, Vb, 3);
                STAMP(20);
                wait_flags(R, fa, tag);
                DRAIN();
                stw2(R, fo1, 0, tag);
                STAMP(21);
                Kb[0] = LDF(nb, 0); Kb[1] = LDF(nb, 1024);
                Vb[0] = LDF(nb, 2048); Vb[1] = LDF(nb, 3072);
                STEP(SB, SA, PB, PA, Ka, 2, Va, 0);
                STEP(SA, SB, PA, PB, Ka, 3, Va, 1);
                Ka[0] = LDF(n2, 0); Ka[1] = LDF(n2, 1024);
                STEP(SB, SA, PB, PA, Kb, 0, Va, 2);
                TCH3();
                STEP(SA, SB, PA, PB, Kb, 1, Va, 3);
                Va[0] = LDF(n2, 2048); Va[1] = LDF(n2, 3072);
                STEP(SB, SA, PB, PA, Kb, 2, Vb, 0);
                W2L(); W2L();
                STEP(SA, SB, PA, PB, Kb, 3, Vb, 1);
                Kb[0] = LDF(n2 + 4096, 0); Kb[1] = LDF(n2 + 4096, 1024);
                STEP(SB, SA, PB, PA, Ka, 0, Vb, 2);
                W2L(); W2L();
            }
            STAMP(18);
            ATT_RUN(n1 - 1, STAMP(32 + 2 * it); if (it == 2) BAR(); STAMP(33 + 2 * it), if (it == 0) { W2L(); W2L(); W2L(); });
            PIN(SA);
            ATT_END();
            PIN(SA);
            if (!grp) BAR();
            STAMP(6);
        }
        {
            lane = fresh_lane();
            vl = lane * 16;
            const int c = lane & 15, g = lane >> 4, q = c >> 2, mb = mt2 * 8 + (wid & 3) * 2;
            wait_flags(R, WS_FH + (((mb * 16 - 2) >> 6) + 1) * 256 + lane * 8, tag);
            STAMP(24);
            const unsigned hso = WS_HS + mb * 32768 + grp * 16384;
            bf16x8 X[8][2];
#pragma unroll
            for (int i = 0; i < 16; ++i) X[i >> 1][i & 1] = LDF(hso + (i & 1) * 32768 + (i >> 3) * 4096, ((i >> 1) & 3) * 1024);
            lds_ptr wb[4];
#pragma unroll
            for (int nb = 0; nb < 4; ++nb)
                wb[nb] = (lds_ptr)smem + grp * 65536 + (4 * g + q) * 128 + ((nb ^ ((2 * g + (q >> 1)) & 3)) << 5) + (lane & 3) * 8;
            LDS_AS f32x4* part = (LDS_AS f32x4*)((lds_ptr)smem + PART + (wid & 3) * 8192) + lane;
            f32x4 a2[2][4] = {};
            if (grp) {
                BAR();
#pragma unroll
                for (int i = 0; i < 8; ++i) a2[i >> 2][i & 3] = part[i * 64];
            }
            STAMP(25);
            bf16x8 W[2][4];
            u32x4 xr[2][2];
#pragma unroll
            for (int nb = 0; nb < 4; ++nb) W[0][nb] = ldtr(wb[nb]);
#pragma unroll
            for (int kb = 0; kb < 16; ++kb) {
                if (kb < 15)
#pragma unroll
                    for (int nb = 0; nb < 4; ++nb) W[(kb + 1) & 1][nb] = ldtr(wb[nb] + (kb + 1) * 4096);
                SBAR();
#pragma unroll
                for (int i = 0; i < 8; ++i) a2[i & 1][i >> 1] = MFMA16(W[kb & 1][i >> 1], X[kb & 7][i & 1], a2[i & 1][i >> 1]);
                SBAR();
                if (kb < 8)
#pragma unroll
                    for (int m = 0; m < 2; ++m) X[kb][m] = LDF(hso + m * 32768 + ((kb + 8) >> 2) * 4096, (kb & 3) * 1024);
                if (kb == 9)
#pragma unroll
                    for (int i = 0; i < 4; ++i)
                        xr[i >> 1][i & 1] = *(const u32x4*)(x + (size_t)((mb + (i >> 1)) * 16 + c) * D_ + nt2 * 64 + (i & 1) * 32 + (g & 1) * 16 + (g >> 1) * 8);
                if (kb == 11) { Ka[0] = LDF(KVO(kt), 0); Ka[1] = LDF(KVO(kt), 1024); Va[0] = LDF(KVO(kt), 2048); Va[1] = LDF(KVO(kt), 3072); }
                if (kb == 13) { Kb[0] = LDF(KVO(kt) + 4096, 0); Kb[1] = LDF(KVO(kt) + 4096, 1024); }
            }
            if (grp) {
#pragma unroll
                for (int m = 0; m < 2; ++m) {
                    float s1 = 0.f, s2 = 0.f;
#pragma unroll
                    for (int p = 0; p < 2; ++p) {
                        auto u = __builtin_amdgcn_permlane16_swap(xr[m][p][0], xr[m][p][2], false, false);
                        auto v = __builtin_amdgcn_permlane16_swap(xr[m][p][1], xr[m][p][3], false, false);
                        const f32x4 lo = {bflo(u[0]), bfhi(u[0]), bflo(v[0]), bfhi(v[0])}, hi = {bflo(u[1]), bfhi(u[1]), bflo(v[1]), bfhi(v[1])};
                        const u32x4 o = pack44(a2[m][2 * p] + lo, a2[m][2 * p + 1] + hi);
#pragma unroll
                        for (int e = 0; e < 4; ++e) { const unsigned w = o[e]; s1 = dot2(w, 0x3F803F80u, s1); s2 = dot2(w, w, s2); }
                        stw4(R, vl, WS_X1 + ((mb + m) * 32 + nt2 * 2 + p) * 1024, o);
                    }
                    s1 = sum32(sum16(s1)); s2 = sum32(sum16(s2));
                    stw2(R, g ? NOWHERE : c * 8, WS_ST + ((mb + m) * 16 + nt2) * 128, (u32x2){__float_as_uint(s1), __float_as_uint(s2)});
                }
                post_flag(R, WS_F2 + (mt2 >> 1) * 2048 + (((mt2 & 1) * 16 + nt2) * 4 + (wid & 3)) * 8, tag, lane);
            } else {
#pragma unroll
                for (int i = 0; i < 8; ++i) part[i * 64] = a2[i >> 2][i & 3];
                LGKM0();
            }
            STAMP(7);
        }
        setupA<true>(L3, lane, wid, lds0, ws + WS_X1 + (size_t)mt3 * 16 * 32768, (const char*)(w3 + nt3 * 128), nullptr);
        const int blk = rt * 4 + ks;
        u32x2 gv;
        {
            Vb[0] = Vb[1] = (bf16x8){};
            ATT_INIT();
            ATT_RUN(7 - n1, STAMP(44 + 2 * it); if (it == 1 && !grp) BAR(); STAMP(45 + 2 * it), );
            const unsigned kvo = KVO(kt), f2o = WS_F2 + mt3 * 2048;
            STAMP(54);
            BAR();
            STAMP(55);
            STEP(SA, SB, PA, PB, Ka, 1, Vb, 3);
            Vb[0] = LDF(kvo + 4096, 2048); Vb[1] = LDF(kvo + 4096, 3072);
            gv = ldw2(R, (lane & 31) * 8, WS_G + blk * 256);
            u32x4 f2 = ldw4(R, vl, f2o);
            STEP(SB, SA, PB, PA, Ka, 2, Va, 0);
            STEP(SA, SB, PA, PB, Ka, 3, Va, 1);
            RETRY((f2[0] ^ tag[0]) | (f2[1] ^ tag[1]) | (f2[2] ^ tag[0]) | (f2[3] ^ tag[1]) | (gv[1] ^ tag[0]),
                  gv = ldw2(R, (lane & 31) * 8, WS_G + blk * 256); f2 = ldw4(R, vl, f2o));
            if (p3_on) { A_ALLB(L3, true, 0, wid); A_ALLA(L3, true, 0); }
            STEP(SB, SA, PB, PA, Kb, 0, Va, 2);
            if (p3_on) { A_ALLB(L3, true, STG_A, wid); A_ALLA(L3, true, STG_A); }
            STEP(SA, SB, PA, PB, Kb, 1, Va, 3);
            STEP(SB, SA, PB, PA, Kb, 2, Vb, 0);
            STEP(SA, SB, PA, PB, Kb, 3, Vb, 1);
            STEPX(false, SB, SA, PB, PA, Ka, 0, Vb, 2);
            ATT_END();
        }
        STAMP(8);
#undef ATT_RUN
#undef ATT_END
#undef ATT_INIT
#undef ATT_FLAG
#undef W2L
#undef TCH3
#undef STEP
#undef STEPX
#undef MM32
#undef MM16
        u32x4 st[4];
        const int srow = (wid * 2 + ((lane >> 3) & 1)) * 16 + (lane & 7) * 2 + (lane >> 5);
#pragma unroll
        for (int i = 0; i < 4; ++i)
            st[i] = ldw4(R, ((lane >> 3) & 1) * 2048 + (lane >> 4) * 512 + (lane & 7) * 16 + i * 128, WS_ST + (mt3 * 16 + wid * 2) * 2048);
        u32x2 og = ldw2(R, srow * 8, WS_G + mt3 * 2048);
        constexpr unsigned CMB = 96 * 1024;
        f32x16 o;
        float l;
        {
            LDS_AS f32x4* me = (LDS_AS f32x4*)((lds_ptr)smem + CMB + wid * 8192) + lane;
#pragma unroll
            for (int i = 0; i < 2; ++i)
#pragma unroll
                for (int e = 0; e < 4; ++e)
                    me[(i * 4 + e) * 64] = (f32x4){O[2 + i][4 * e], O[2 + i][4 * e + 1], O[2 + i][4 * e + 2], O[2 + i][4 * e + 3]};
            LG_BAR();
            LDS_AS f32x4* pa = (LDS_AS f32x4*)((lds_ptr)smem + CMB + (wid ^ 4) * 8192) + lane;
#pragma unroll
            for (int i = 1; i >= 0; --i)
#pragma unroll
                for (int e = 0; e < 4; ++e) {
                    const f32x4 v = pa[(i * 4 + e) * 64];
#pragma unroll
                    for (int j = 0; j < 4; ++j) O[i][4 * e + j] += v[j];
                }
#pragma unroll
            for (int e = 0; e < 4; ++e) pa[e * 64] = (f32x4){O[1][4 * e], O[1][4 * e + 1], O[1][4 * e + 2], O[1][4 * e + 3]};
            pa[256] = Ls;
            SBAR();
            RETRY(og[1] ^ tag[0], og = ldw2(R, srow * 8, WS_G + mt3 * 2048));
            const f32x4 sv = (__builtin_bit_cast(f32x4, st[0]) + __builtin_bit_cast(f32x4, st[1])) +
                             (__builtin_bit_cast(f32x4, st[2]) + __builtin_bit_cast(f32x4, st[3]));
            auto p1 = __builtin_amdgcn_permlane32_swap(__float_as_uint(sv[0]), __float_as_uint(sv[2]), false, false);
            auto p2 = __builtin_amdgcn_permlane32_swap(__float_as_uint(sv[1]), __float_as_uint(sv[3]), false, false);
            const float mean = sum16(__uint_as_float(p1[0]) + __uint_as_float(p1[1])) * (1.0f / D_);
            const float var = sum16(__uint_as_float(p2[0]) + __uint_as_float(p2[1])) * (1.0f / D_) - mean * mean;
            const float r = __builtin_amdgcn_rsqf(__builtin_fmaxf(var, 0.f) + 1e-5f);
            f32x4 rc = {mean, GC0 * r, GC1 * r * r * r, __builtin_fminf(__builtin_amdgcn_rcpf(r * bfhi(og[0])), 1e30f)};
            PIN(rc);
            LG_BAR();
            const LDS_AS f32x4* p6 = (const LDS_AS f32x4*)((lds_ptr)smem + CMB + (wid ^ 6) * 8192) + lane;
#pragma unroll
            for (int e = 0; e < 4; ++e) {
                const f32x4 v = p6[e * 64];
#pragma unroll
                for (int j = 0; j < 4; ++j) o[4 * e + j] = O[0][4 * e + j] + v[j];
            }
            l = (Ls[0] + ((const LDS_AS float*)(p6 + 256))[1]) +
                (((const LDS_AS float*)(me + 256))[2] + ((const LDS_AS float*)((lds_ptr)smem + CMB + (wid ^ 2) * 8192 + 4096 + vl))[3]);
            *(LDS_AS f32x4*)((lds_ptr)smem + RCL + grp * 8192 + (srow & 127) * 16) = rc;
        }
        {
            float n = 0.f;
#pragma unroll
            for (int r = 0; r < 16; ++r) n = __builtin_fmaf(o[r], 0.f, n);
            const bool ok = l >= 0x1p-50f && l <= 0x1p50f && n == 0.f;
            if (__builtin_expect(!__all(ok), 0)) {
                const bf16x8 q0 = Q[0][0], q1 = Q[0][1];
                wait_flags(R, WS_F1 + (head * 32 + (lane & 31)) * 8, tag);
                float m = -INFINITY;
#pragma unroll 1
                for (int t = 0; t < 64; ++t) {
                    f32x16 s = MFMA32(LDF(kvh + t * 4096, 0), q0, (f32x16){});
                    s = MFMA32(LDF(kvh + t * 4096, 1024), q1, s);
#pragma unroll
                    for (int r = 0; r < 16; ++r) m = __builtin_fmaxf(m, s[r]);
                }
                m = max32(m);
                o = (f32x16){};
                l = 0.f;
#pragma unroll 1
                for (int t = 0; t < 64; ++t) {
                    f32x16 s = MFMA32(LDF(kvh + t * 4096, 0), q0, (f32x16){});
                    s = MFMA32(LDF(kvh + t * 4096, 1024), q1, s);
                    u32x4 p[2];
#pragma unroll
                    for (int r = 0; r < 8; ++r) {
                        const unsigned w = cvtpk(__builtin_amdgcn_exp2f(s[2 * r] - m), __builtin_amdgcn_exp2f(s[2 * r + 1] - m));
                        p[r >> 2][r & 3] = w;
                        l += bflo(w) + bfhi(w);
                    }
                    o = MFMA32(LDF(kvh + t * 4096, 2048), BC8(p[0]), o);
                    o = MFMA32(LDF(kvh + t * 4096, 3072), BC8(p[1]), o);
                }
                l = sum32(l);
            }
        }
        {
            const float sc = bflo(gv[0]) / l;
            const unsigned vo = ((lane >> 4) & 1) * (144 * 1024) + ((lane >> 5) * 16 + (lane & 15)) * 16;
#pragma unroll
            for (int u = 0; u < 2; ++u)
                stw4(R, vo + u * 512, WS_A4 + (blk * 2 * 144 + (head >> 4) * 72 + (head & 15)) * 1024,
                     pack44((f32x4){o[4 * u], o[4 * u + 1], o[4 * u + 2], o[4 * u + 3]} * sc,
                            (f32x4){o[8 + 4 * u], o[9 + 4 * u], o[10 + 4 * u], o[11 + 4 * u]} * sc));
        }
        L3.post = WS_FO + (((blk >> 2) * 2 + (head >> 4)) * 64 + (head & 15) * 4 + (blk & 3)) * 8;
        STAMP(9);
#undef LDF
#undef KVO
#undef EX
#undef CV
    }
    {
        const int lane = fresh_lane(), c = lane & 15, g = lane >> 4, q = c >> 2;
        const int kh = xcd & 1, mt4 = 8 * ((xcd >> 1) & 1) + (jb & 7), nt4 = 4 * (xcd >> 2) + (jb >> 3);
        const int j = wid & 3, wm = j & 1, wn = j >> 1;
        constexpr unsigned STG_B = 32768;
        const unsigned xv0 = (2 * (j ^ grp ^ 1) + kh) * (144 * 1024) + lane * 16 + 2048;
        const unsigned xv1 = (2 * (j ^ grp ^ 1) + (kh ^ 1)) * (144 * 1024) + lane * 16;
        const int ir = 16 * j + (lane >> 3);
        const unsigned wva = ir * 2048 + (((lane & 7) ^ (((ir >> 1) & 3) << 1)) << 4), wvb = wva + 8 * 2048 - 1024;
        const char* xs4 = ws + WS_A4 + ((size_t)mt4 * 8 * 144 + kh * 72) * 1024 - 2048;
        const char* wo4 = (const char*)(wo + (size_t)kh * 512 * D_ + nt4 * 128);
        const char* w44 = (const char*)(w4 + (size_t)kh * 1792 * D_ + nt4 * 128) - 8 * 131072;
        const unsigned dx4 = lds0 + j * 4096, dw4 = lds0 + 16384 + j * 2048;
#define B_SET(tile)                                                                          \
    const int tl_ = (tile);                                                                  \
    const char* bx_ = xs4 + tl_ * 2048;                                                      \
    const char* bw_ = (tl_ < 8 ? wo4 : w44) + tl_ * 131072;                                  \
    const char* bw1_ = bw_ + 128;                                                            \
    const unsigned bo_ = (tl_ & 3) * STG_B
#define B_X0() GLDS(dx4 + bo_, xv0, bx_, 0, " sc1")
#define B_X1() GLDS(dx4 + bo_, xv0, bx_, 1024, " sc1")
#define B_X2() GLDS(dx4 + bo_, xv1, bx_, 2048, " sc1")
#define B_X3() GLDS(dx4 + bo_, xv1, bx_, 3072, " sc1")
#define B_W0() GLDS(dw4 + bo_, wva, bw_, 0, "")
#define B_W1() GLDS(dw4 + bo_, wvb, bw_, 1024, "")
#define B_W2() GLDS(dw4 + 8192 + bo_, wva, bw1_, 0, "")
#define B_W3() GLDS(dw4 + 8192 + bo_, wvb, bw1_, 1024, "")
#define B_ALL(tile) do { B_SET(tile); B_W0(); B_W1(); B_W2(); B_W3(); B_X0(); B_X1(); B_X2(); B_X3(); } while (0)
        const unsigned foo = WS_FO + (mt4 * 2 + kh) * 512 + lane * 8;
        if (p3_on) {
            setupR<true>(L3, smem, lane, wid);
            f32x4 acc[4][4] = {};
#pragma unroll
            for (int i = 0; i < 16; ++i) PIN(acc[i >> 2][i & 3]);
            WAIT_BAR(8);
            if (grp) BAR();
            STAMP(10);
            u32x2 fo;
            kloopA<2>(ws, tag, smem, L3, lane, wid, acc, [&]() { STAMP(27); }, [&]() { fo = ldw2(R, foo, 0); });
            STAMP(11);
            if (grp) LG_BAR();
            if (any(diff(fo, tag))) wait_flags(R, foo, tag);
            if (grp) { B_ALL(0); B_ALL(2); } else { B_ALL(1); }
            f32x4 cs[4];
            read_cs(smem, lane, wid, cs);
            const unsigned a4o = WS_A4 + ((mt3 * 16 + j * 4) * 144 + (nt3 / 14) * 72 + 16 + (nt3 % 14) * 4 + grp * 2) * 1024;
#pragma unroll
            for (int mb = 0; mb < 4; ++mb) {
                const f32x4 rc = *(const LDS_AS f32x4*)((lds_ptr)smem + RCL + (j >> 1) * 8192 + ((j & 1) * 64 + mb * 16 + c) * 16);
#pragma unroll
                for (int p = 0; p < 2; ++p)
                    stw4(R, lane * 16, a4o + (mb * 144 + (p ^ (j >> 1))) * 1024,
                         ln_gelu_chunk(acc[2 * p][mb], acc[2 * p + 1][mb], cs[2 * p], cs[2 * p + 1], rc[0], rc[1], rc[2], rc[3]));
                if (mb == 0) { SBAR(); if (!grp) BAR(); SBAR(); }
            }
            post_flag(R, WS_F3 + (((mt3 * 2 + (j >> 1)) * 2 + nt3 / 14) * 64 + (nt3 % 14) * 4 + grp * 2 + (j & 1)) * 8, tag, lane);
        } else {
            post_flag(R, L3.post, tag, lane);
            const int idx = (xcd * 4 + jb - 28) * 8 + wid;
            unsigned scrap = 0;
            asm volatile("global_load_dword %0, %1, %2" : "+v"(scrap) : "v"((idx * 64 + lane) * 128), "s"(wo) : "memory");
#pragma unroll 1
            for (int i = 0; i < 4; ++i) {
                __builtin_amdgcn_s_sleep(24);
                const unsigned vo = ((((i & 1) * 2 + (i >> 1)) * 256 + idx) * 56 + (lane < 56 ? lane : 55)) * 128;
                asm volatile("global_load_dword %0, %1, %2" : "+v"(scrap) : "v"(vo), "s"(w4) : "memory");
            }
            asm volatile("s_waitcnt vmcnt(0)" : "+v"(scrap)::"memory");
            wait_flags(R, foo, tag);
            if (grp) { B_ALL(0); B_ALL(2); } else { B_ALL(1); }
            DRAIN();
        }
        STAMP(12);
        BAR();
        if (grp) BAR();
        STAMP(13);
        lds_ptr xb = (lds_ptr)smem + grp * STG_B + (8 * wm) * 1024 + lane * 16, wb[4];
#pragma unroll
        for (int nb = 0; nb < 4; ++nb)
            wb[nb] = (lds_ptr)smem + grp * STG_B + 16384 + wn * 8192 + (4 * g + q) * 128 + ((nb ^ ((2 * g + (q >> 1)) & 3)) << 5) + (lane & 3) * 8;
        f32x4 acc[4][4] = {};
        const unsigned f3o = WS_F3 + (mt4 * 2 + kh) * 512 + (lane < 56 ? lane : 0) * 8;
        const int mblk = mt4 * 8 + wm * 4 + grp * 2 + kh;
        const unsigned go = WS_G + mblk * 128 + c * 8;
        const char *bx = xs4 + (3 + grp) * 2048, *bw = wo4 + (3 + grp) * 131072;
        unsigned bo = (3 - 3 * grp) * STG_B;
        int adv = 2 * STG_B;
        u32x2 f3 = {0u, 0u};
        auto body = [&](auto kind) {
            constexpr int KIND = decltype(kind)::value;
            bf16x8 xf[4][2], wf[4][2];
            const char* bw1 = bw + 128;
            SBAR();
            if (KIND == 0) asm volatile("global_load_dwordx2 %0, %1, %2 sc1" : "+v"(f3) : "v"(f3o), "s"(ws) : "memory");
            wf[0][0] = ldtr(wb[0]); xf[0][0] = ld128(xb); SBAR();
            if (KIND == 0) { M0SET(dx4 + bo); GL(xv0, bx, 0, " sc1"); SBAR(); }
            wf[1][0] = ldtr(wb[1]); xf[1][0] = ld128(xb + 2048); SBAR();
            if (KIND == 0) { GL(xv0, bx, 1024, " sc1"); SBAR(); }
            wf[2][0] = ldtr(wb[2]); xf[2][0] = ld128(xb + 4096); SBAR();
            if (KIND == 0) { GL(xv1, bx, 2048, " sc1"); SBAR(); }
            wf[3][0] = ldtr(wb[3]); xf[3][0] = ld128(xb + 6144); SBAR();
            if (KIND == 0) { GL(xv1, bx, 3072, " sc1"); SBAR(); }
            wf[0][1] = ldtr(wb[0] + 4096); xf[0][1] = ld128(xb + 1024); SBAR();
            if (KIND == 0) { M0SET(dw4 + bo); GL(wva, bw, 0, ""); SBAR(); }
            wf[1][1] = ldtr(wb[1] + 4096); xf[1][1] = ld128(xb + 3072); SBAR();
            if (KIND == 0) { GL(wvb, bw, 1024, ""); SBAR(); }
            wf[2][1] = ldtr(wb[2] + 4096); xf[2][1] = ld128(xb + 5120); SBAR();
            if (KIND == 0) { M0SET(dw4 + 8192 + bo); GL(wva, bw1, 0, ""); SBAR(); }
            wf[3][1] = ldtr(wb[3] + 4096); xf[3][1] = ld128(xb + 7168); SBAR();
            if (KIND == 0) {
                GL(wvb, bw1, 1024, "");
                bx += 4096; bw += 2 * 131072; bo ^= 2 * STG_B;
                asm volatile("" : "+s"(bx), "+s"(bw), "+s"(bo));
                SBAR();
                WAIT_BAR(9);
            } else {
                WAIT_BAR(0);
            }
            SBAR();
#pragma unroll
            for (int i = 0; i < 32; ++i) {
                acc[i & 3][(i >> 2) & 3] = MFMA16(wf[i & 3][i >> 4], xf[(i >> 2) & 3][i >> 4], acc[i & 3][(i >> 2) & 3]);
                if (i == 19) { xb += adv; PIN(xb); }
                if (i >= 20 && i < 24) { wb[i & 3] += adv; PIN(wb[i & 3]); }
                SBAR();
            }
            if (KIND == 0) BAR();
            SBAR();
            adv = -adv;
        };
#pragma unroll 1
        for (int seg = 0, n = 3 - grp; seg < 2; ++seg, n = 14) {
#pragma unroll 1
            for (int t = 0; t < n; ++t) {
                body(IC<0>{});
            }
            asm volatile("s_waitcnt vmcnt(8)" : "+v"(f3)::"memory");
            if (seg == 0) {
                if (any(diff(f3, tag))) wait_flags(R, f3o, tag);
                bw = w44 + (9 - grp) * 131072;
            }
        }
#pragma unroll 1
        for (int t = 0; t <= grp; ++t) {
            body(IC<1>{});
            if (t == 0) BAR();
        }
        u32x4 xr[2];
        xr[0] = ldw4(R, lane * 16, WS_X1 + (mblk * 32 + nt4 * 4 + wn * 2) * 1024);
        xr[1] = ldw4(R, lane * 16 + 1024, WS_X1 + (mblk * 32 + nt4 * 4 + wn * 2) * 1024);
        u32x2 og = ldw2(R, go, 0);
        STAMP(14);
        f32x4 res[4][2];
        {
            LDS_AS f32x4* ex = (LDS_AS f32x4*)((lds_ptr)smem + wid * 8192) + lane;
            const LDS_AS f32x4* ey = (const LDS_AS f32x4*)((lds_ptr)smem + (wid ^ 4) * 8192) + lane;
#pragma unroll
            for (int i = 0; i < 8; ++i) ex[i * 64] = acc[i & 3][2 + (i >> 2)];
            LG_BAR();
#pragma unroll
            for (int i = 0; i < 8; ++i) res[i & 3][i >> 2] = ey[i * 64] + acc[i & 3][i >> 2];
        }
        const int me = (mt4 * 8 + nt4) * 2 + kh;
#pragma unroll
        for (int nb = 0; nb < 4; ++nb)
            stw4(R, lane * 16 + nb * 1024, WS_EX + me * 32768 + wid * 4096, __builtin_bit_cast(u32x4, res[nb][1]));
        post_flag(R, WS_FX + (me * 8 + wid) * 8, tag, lane);
        STAMP(15);
        wait_flags(R, WS_FX + ((me ^ 1) * 8 + wid) * 8, tag);
        STAMP(16);
        u32x4 pv[4];
#pragma unroll
        for (int nb = 0; nb < 4; ++nb) pv[nb] = ldw4(R, lane * 16 + nb * 1024, WS_EX + (me ^ 1) * 32768 + wid * 4096);
#pragma unroll 1
        for (unsigned n = 0; n < SPIN_CAP; ++n) {
            if (!any(og[1] ^ tag[0])) break;
            __builtin_amdgcn_s_sleep(2);
            asm volatile("" ::: "memory");
            og = ldw2(R, go, 0);
        }
        const float omg = bfhi(og[0]);
#pragma unroll
        for (int p = 0; p < 2; ++p) {
            const f32x4 lo = {bflo(xr[p][0]), bfhi(xr[p][0]), bflo(xr[p][1]), bfhi(xr[p][1])};
            const f32x4 hi = {bflo(xr[p][2]), bfhi(xr[p][2]), bflo(xr[p][3]), bfhi(xr[p][3])};
            const u32x4 o = pack44(lo * omg + (res[2 * p][0] + __builtin_bit_cast(f32x4, pv[2 * p])),
                                   hi * omg + (res[2 * p + 1][0] + __builtin_bit_cast(f32x4, pv[2 * p + 1])));
            auto u = __builtin_amdgcn_permlane16_swap(o[0], o[2], false, false);
            auto v = __builtin_amdgcn_permlane16_swap(o[1], o[3], false, false);
            *(u32x4*)(out + (size_t)(mblk * 16 + c) * D_ + nt4 * 128 + wn * 64 + p * 32 + (g & 1) * 16 + (g >> 1) * 8) =
                (u32x4){u[0], v[0], u[1], v[1]};
        }
        if (b == 0 && wid == 0) stw2(R, lane ? NOWHERE : 0, WS_EPOCH, tag);
        STAMP(17);
        STAMP_OUT();
    }
}

extern "C" void kernel_launch(void* const* d_in, const int* in_sizes, int n_in, void* d_out, int out_size, void* d_ws,
                              size_t ws_size, hipStream_t stream) {
    (void)hipFuncSetAttribute((const void*)k_mega, hipFuncAttributeMaxDynamicSharedMemorySize, LDS_BYTES);
    k_mega<<<256, 512, LDS_BYTES, stream>>>((const u16*)d_in[0], (const u16*)d_in[1], (const u16*)d_in[2], (const u16*)d_in[4],
                                             (const u16*)d_in[5], (const u16*)d_in[6], (const u16*)d_in[7], (const u16*)d_in[8],
                                             (char*)d_ws, (u16*)d_out);
}
```

```cpp
#include <hip/hip_runtime.h>
#include <hip/hip_bf16.h>
#include <stdint.h>

typedef unsigned short u16;
typedef unsigned long long u64;
typedef short bf16x8 __attribute__((ext_vector_type(8)));
typedef short v4i16_t __attribute__((ext_vector_type(4)));
typedef __bf16 bf2_t __attribute__((ext_vector_type(2)));
typedef float f32x16 __attribute__((ext_vector_type(16)));
typedef float f32x4 __attribute__((ext_vector_type(4)));
typedef float f32x2 __attribute__((ext_vector_type(2)));
typedef unsigned u32x4 __attribute__((ext_vector_type(4)));
typedef unsigned u32x2 __attribute__((ext_vector_type(2)));
typedef __attribute__((address_space(3))) char* lds_ptr;
typedef __amdgpu_buffer_rsrc_t rsrc_t;
#define DEV __device__ __forceinline__
#define LDS_AS __attribute__((address_space(3)))
#define SBAR() __builtin_amdgcn_sched_barrier(0)
#define PIN(x) asm volatile("" : "+v"(x))
#define MFMA32(a, b, c) __builtin_amdgcn_mfma_f32_32x32x16_bf16(a, b, c, 0, 0, 0)
#define MFMA16(a, b, c) __builtin_amdgcn_mfma_f32_16x16x32_bf16(a, b, c, 0, 0, 0)
#define BAR() asm volatile("s_barrier" ::: "memory")
#define LGKM0() __builtin_amdgcn_s_waitcnt(0xC07F)
#define WAIT_BAR(N) do { LGKM0(); asm volatile("s_waitcnt vmcnt(" #N ")\n\ts_barrier" ::: "memory"); } while (0)
#define LG_BAR() do { asm volatile("" ::: "memory"); LGKM0(); asm volatile("s_barrier" ::: "memory"); } while (0)
#define DRAIN() asm volatile("s_waitcnt vmcnt(0)" ::: "memory")
#define BC8(x) __builtin_bit_cast(bf16x8, x)

constexpr int F1S = 128, N1A = 2, N1B = 6;
template <int N> struct IC { static constexpr int value = N; };
constexpr int S_ = 2048, D_ = 1024, F_ = 3584;
constexpr unsigned KB = 1024, MB = 1u << 20;
constexpr unsigned WS_EPOCH = 0;
constexpr unsigned WS_F1 = 1 * MB;
constexpr unsigned WS_FH = 12 * KB;
constexpr unsigned WS_F2 = 24 * KB;
constexpr unsigned WS_FO = 40 * KB;
constexpr unsigned WS_F3 = 56 * KB;
constexpr unsigned WS_FX = 72 * KB;
constexpr unsigned WS_RS = 128 * KB;
constexpr unsigned WS_G = 160 * KB;
constexpr unsigned WS_ST = 256 * KB;
constexpr unsigned WS_KV = 5 * MB;
constexpr unsigned WS_HS = 13 * MB;
constexpr unsigned WS_X1 = 17 * MB;
constexpr unsigned WS_A4 = 21 * MB;
constexpr unsigned WS_EX = 40 * MB;
constexpr int LDS_BYTES = 160 * 1024;
constexpr unsigned SPIN_CAP = 1u << 18;

DEV unsigned cvtpk(float lo, float hi) {
    const f32x2 v = {lo, hi};
    return __builtin_bit_cast(unsigned, __builtin_convertvector(v, bf2_t));
}
DEV float bflo(unsigned u) { return __uint_as_float(u << 16); }
DEV float bfhi(unsigned u) { return __uint_as_float(u & 0xffff0000u); }
DEV float rbf(float f) { return bflo(cvtpk(f, f)); }
DEV float dot2(unsigned a, unsigned b, float c) {
    return __builtin_amdgcn_fdot2_f32_bf16(__builtin_bit_cast(bf2_t, a), __builtin_bit_cast(bf2_t, b), c, false);
}
DEV u32x4 pack44(const f32x4& a, const f32x4& b) {
    return (u32x4){cvtpk(a[0], a[1]), cvtpk(a[2], a[3]), cvtpk(b[0], b[1]), cvtpk(b[2], b[3])};
}
DEV float sum32(float v) {
    auto r = __builtin_amdgcn_permlane32_swap(__float_as_uint(v), __float_as_uint(v), false, false);
    return __uint_as_float(r[0]) + __uint_as_float(r[1]);
}
DEV float sum16(float v) {
    auto r = __builtin_amdgcn_permlane16_swap(__float_as_uint(v), __float_as_uint(v), false, false);
    return __uint_as_float(r[0]) + __uint_as_float(r[1]);
}
DEV float max32(float v) {
    auto r = __builtin_amdgcn_permlane32_swap(__float_as_uint(v), __float_as_uint(v), false, false);
    return __builtin_fmaxf(__uint_as_float(r[0]), __uint_as_float(r[1]));
}
template <int CTRL>
DEV float dpp_add(float v) {
    return v + __uint_as_float(__builtin_amdgcn_update_dpp(0u, __float_as_uint(v), CTRL, 0xf, 0xf, true));
}
DEV float wave_sum(float v) {
    v = dpp_add<0xB1>(v);
    v = dpp_add<0x4E>(v);
    v = dpp_add<0x141>(v);
    v = dpp_add<0x140>(v);
    return sum32(sum16(v));
}
DEV bf16x8 ld128(lds_ptr p) { return *(const LDS_AS bf16x8*)p; }
template <int H2 = 2048>
DEV bf16x8 ldtr(lds_ptr p) {
    v4i16_t a = __builtin_amdgcn_ds_read_tr16_b64_v4i16((LDS_AS v4i16_t*)p);
    v4i16_t b = __builtin_amdgcn_ds_read_tr16_b64_v4i16((LDS_AS v4i16_t*)(p + H2));
    return (bf16x8){a[0], a[1], a[2], a[3], b[0], b[1], b[2], b[3]};
}
DEV int rfl(int v) { return __builtin_amdgcn_readfirstlane(v); }
DEV int fresh_lane() {
    int l;
    asm volatile("v_mbcnt_lo_u32_b32 %0, -1, 0\n\tv_mbcnt_hi_u32_b32 %0, -1, %0" : "=v"(l));
    __builtin_assume(l >= 0 && l < 64);
    return l;
}
DEV u32x4 ldw4(rsrc_t r, unsigned v, unsigned s) { return __builtin_amdgcn_raw_buffer_load_b128(r, v, s, 16); }
DEV u32x2 ldw2(rsrc_t r, unsigned v, unsigned s) { return __builtin_amdgcn_raw_buffer_load_b64(r, v, s, 16); }
DEV void stw4(rsrc_t r, unsigned v, unsigned s, u32x4 x) {
    __builtin_amdgcn_raw_buffer_store_b128(x, r, v, s, 16);
    asm volatile("s_nop 1" ::"v"(x) : "memory");
}
DEV void stw2(rsrc_t r, unsigned v, unsigned s, u32x2 x) { __builtin_amdgcn_raw_buffer_store_b64(x, r, v, s, 16); }
constexpr unsigned NOWHERE = 0x80000000u;
#define GLDS(dst, voff, sptr, IMM, SC)                                                                        \
    asm volatile("s_mov_b32 m0, %0\n\ts_nop 0\n\tglobal_load_lds_dwordx4 %1, %2 offset:" #IMM SC ::"s"(dst), "v"(voff), \
                 "s"(sptr) : "memory")
#define IF_EQ(t, K) "s_cmp_lg_u32 " t ", " #K "\n\ts_cbranch_scc1 .Lskip%=\n\t"
#define END_IF "\n.Lskip%=:"
DEV void post_flag(rsrc_t r, unsigned off, u32x2 tag, int lane) {
    DRAIN();
    stw2(r, lane == 0 ? 0 : NOWHERE, off, tag);
}
DEV bool any(unsigned v) { return __builtin_amdgcn_ballot_w64(v != 0) != 0; }
DEV unsigned diff(u32x2 v, u32x2 tag) { return (v[0] ^ tag[0]) | (v[1] ^ tag[1]); }
DEV void wait_flags(rsrc_t r, unsigned voff, u32x2 tag) {
#pragma unroll 1
    for (unsigned n = 0; n < SPIN_CAP; ++n) {
        asm volatile("" ::: "memory");
        const u32x2 v = ldw2(r, voff, 0);
        if (!any(diff(v, tag))) break;
        __builtin_amdgcn_s_sleep(2);
    }
    asm volatile("" ::: "memory");
}
#define RETRY(BAD, LOADS)                                                    \
    if (__builtin_expect(any(BAD), 0)) {                                     \
        _Pragma("unroll 1") for (unsigned n_ = 0; n_ < SPIN_CAP; ++n_) {     \
            __builtin_amdgcn_s_sleep(2);                                     \
            asm volatile("" ::: "memory");                                   \
            LOADS;                                                           \
            __builtin_amdgcn_s_waitcnt(0x0F70);                              \
            if (!any(BAD)) break;                                            \
        }                                                                    \
    }
DEV f32x2 gelu2(f32x2 v, float a, float b, float k) {
    const f32x2 y = v * __builtin_elementwise_fma(v * v, (f32x2){b, b}, (f32x2){a, a});
    const f32x2 e = {__builtin_amdgcn_exp2f(y[0]), __builtin_amdgcn_exp2f(y[1])};
    const f32x2 d = __builtin_elementwise_fma(e, (f32x2){k, k}, (f32x2){k, k});
    return v * (f32x2){__builtin_amdgcn_rcpf(d[0]), __builtin_amdgcn_rcpf(d[1])};
}
constexpr float GC0 = -2.0f * 0.7978845608028654f * 1.4426950408889634f, GC1 = GC0 * 0.044715f;
DEV u32x4 ln_gelu_chunk(const f32x4& p, const f32x4& q, const f32x4& cp, const f32x4& cq, float mean, float a, float b, float k) {
    const f32x2 nm = {-mean, -mean};
    const f32x2 r0 = gelu2(__builtin_elementwise_fma(nm, (f32x2){cp[0], cp[1]}, (f32x2){p[0], p[1]}), a, b, k);
    const f32x2 r1 = gelu2(__builtin_elementwise_fma(nm, (f32x2){cp[2], cp[3]}, (f32x2){p[2], p[3]}), a, b, k);
    const f32x2 r2 = gelu2(__builtin_elementwise_fma(nm, (f32x2){cq[0], cq[1]}, (f32x2){q[0], q[1]}), a, b, k);
    const f32x2 r3 = gelu2(__builtin_elementwise_fma(nm, (f32x2){cq[2], cq[3]}, (f32x2){q[2], q[3]}), a, b, k);
    return (u32x4){cvtpk(r0[0], r0[1]), cvtpk(r1[0], r1[1]), cvtpk(r2[0], r2[1]), cvtpk(r3[0], r3[1])};
}

constexpr unsigned STG_A = 49152, CSL = 3 * STG_A;
constexpr unsigned RCL = 150 * 1024;
constexpr unsigned PART = 128 * 1024;
struct LoadA {
    const char *a, *a3, *b;
    unsigned va[4], vb[2], da;
    u64 ta;
    unsigned tstep, scrap;
    unsigned post;
    lds_ptr xb[2], wb[4], wb1[4];
};
template <bool FO>
DEV void setupA(LoadA& L, int lane, int wid, unsigned lds0, const char* xs, const char* w0, const char* w1) {
    const int ir = 8 * wid + (lane >> 3);
    const unsigned wch = ((lane & 7) ^ (((ir >> 1) & 3) << 1)) << 4;
    if (FO) {
        L.a = L.a3 = xs; L.b = w0 - 896;
        L.va[0] = L.va[1] = (2 * wid) * 32768 + lane * 16;
        L.va[2] = L.va[3] = L.va[0] + 32768 - 2048;
        L.vb[1] = ir * (F_ * 2) + wch;
        L.vb[0] = L.vb[1] + 896;
    } else {
        const int sr = (ir & 32) | ((ir & 12) << 1) | ((ir & 16) >> 2) | (ir & 3);
        L.a = w0; L.a3 = w1 - 3072; L.b = xs;
        L.va[0] = sr * 6144 + wch; L.va[1] = L.va[0] + 1024; L.va[2] = L.va[0] + 2048; L.va[3] = sr * 2048 + wch;
#pragma unroll
        for (int i = 0; i < 2; ++i) {
            const int row = 16 * wid + 8 * i + (lane >> 3);
            L.vb[i] = row * 2048 + (((lane & 7) ^ ((row >> 1) & 7)) << 4) - i * 1024;
        }
    }
    L.da = lds0 + wid * 4096;
}
template <bool FO>
DEV void setupR(LoadA& L, char* smem, int lane, int wid) {
    const int c = lane & 15, g = lane >> 4, q = c >> 2, grp = wid >> 2;
    const int sx = (FO || grp) ? (wid & 1) : 0, sw = (2 * g + (q >> 1)) & 3;
    const lds_ptr wr = (lds_ptr)smem + ((4 * g + q) & 7) * 128 + (lane & 3) * 8;
#pragma unroll
    for (int s = 0; s < 2; ++s) {
        const int sa = s ^ sx;
        L.xb[s] = (lds_ptr)smem + (FO ? (8 * (wid & 3) + sa) * 1024 + lane * 16
                                      : 32768 + (64 * (wid & 1) + c) * 128 + (((4 * sa + g) ^ ((c >> 1) & 7)) << 4));
        PIN(L.xb[s]);
    }
#pragma unroll
    for (int nb = 0; nb < (FO ? 4 : 2); ++nb) {
        L.wb[nb] = wr + (((nb ^ (wid & 2)) ^ sw) << 5) +
                   (FO ? 32768 + grp * 1024 + (g >> 1) * 2048 + sx * 8192 : (1 - grp) * 1024 + (g >> 1) * 4096 + sx * 16384);
        L.wb1[nb] = L.wb[nb] + (FO ? 8192 : 16384) * (1 - 2 * sx);
        PIN(L.wb[nb]); PIN(L.wb1[nb]);
    }
}
#define M0SET(v) asm volatile("s_mov_b32 m0, %0\n\ts_nop 0" ::"s"(v) : "memory")
#define GL(voff, sptr, IMM, SC) asm volatile("global_load_lds_dwordx4 %0, %1 offset:" #IMM SC ::"v"(voff), "s"(sptr) : "memory")
#define A_SETA(L, so) M0SET(L.da + (so))
#define A_SETB(L, so, wid) M0SET(L.da + 32768 - (wid) * 2048 + (so))
#define A_A(L, FO, i, IMM) do { if (FO) GL(L.va[i], L.a, IMM, " sc1"); else if (i < 3) GL(L.va[i], L.a, IMM, ""); else GL(L.va[i], L.a3, IMM, ""); } while (0)
#define A_ALLA(L, FO, so) do { A_SETA(L, so); A_A(L, FO, 0, 0); A_A(L, FO, 1, 1024); A_A(L, FO, 2, 2048); A_A(L, FO, 3, 3072); A_NEXTA(L, FO); } while (0)
#define A_ALLB(L, FO, so, wid) do { A_SETB(L, so, wid); GL(L.vb[0], L.b, 0, ""); GL(L.vb[1], L.b, 1024, ""); A_NEXTB(L, FO); } while (0)
#define A_NEXTA(L, FO) do { L.a += FO ? 2048 : 64 * 6144; if (!FO) L.a3 += 64 * 2048; } while (0)
#define A_NEXTB(L, FO) L.b += FO ? 64 * F_ * 2 : 128
#define A_TOUCH(L) asm volatile("global_load_dword %0, %1, off" : "+v"(L.scrap) : "v"(L.ta) : "memory")
#define TCH(step)                                                                                               \
    do {                                                                                                        \
        asm volatile("s_load_dword %0, %1, 0x0\n\ts_load_dword %0, %1, 0x40\n\ts_load_dword %0, %1, 0x80\n\t"  \
                     "s_load_dword %0, %1, 0xc0\n\ts_load_dword %0, %1, 0x100\n\ts_load_dword %0, %1, 0x140\n\t" \
                     "s_load_dword %0, %1, 0x180\n\ts_load_dword %0, %1, 0x1c0" : "+s"(scrap) : "s"(t3));       \
        t3 += step;                                                                                             \
    } while (0)
#define POST_BAR(N, N0, t, off, tag, ws)                                                                       \
    do {                                                                                                       \
        LGKM0();                                                                                               \
        asm volatile("s_cmp_eq_u32 %0, 0\n\ts_waitcnt vmcnt(" #N0 ")\n\ts_cbranch_scc1 .Lskip%=\n\t"          \
                     "s_cmp_lg_u32 %0, 1\n\ts_waitcnt vmcnt(" #N ")\n\ts_cbranch_scc1 .Lskip%=\n\t"           \
                     "s_mov_b64 exec, 1\n\tglobal_store_dwordx2 %1, %2, %3 sc1\n\ts_mov_b64 exec, -1"         \
                     "\n.Lskip%=:\n\ts_add_u32 %0, %0, 1\n\ts_barrier"                                        \
                     : "+s"(t) : "v"(off), "v"(tag), "s"(ws) : "memory", "scc");                               \
    } while (0)

template <int mode, class Hook, class Hook2>
DEV void kloopA(char* ws, u32x2 tag, char* smem, LoadA& L, int lane, int wid, f32x4 (&acc)[4][4], Hook hook, Hook2 hook2) {
    constexpr bool FO = mode == 2;
    constexpr int H2 = FO ? 4096 : 8192, NP = FO ? 4 : 2;
    float cs0 = 0.f, cs1 = 0.f, cs2 = 0.f, cs3 = 0.f;
    unsigned so2 = 2 * STG_A, tm = ~0u;
    int tc = FO ? 0 : -11;
    if (FO) { PIN(tag); PIN(L.post); }
    auto body = [&](auto kind) {
        constexpr int KIND = decltype(kind)::value;
        bf16x8 xf[4][2], wf[4][2];
#define RD(i, s)                                                                                                     \
    wf[i][s] = ldtr<H2>((s && mode ? L.wb1[i % NP] : L.wb[i % NP]) + (s && !mode ? 16384 : 0) + (i / NP) * (mode ? 3072 : 1024)); \
    xf[i][s] = ld128(L.xb[s] + i * 2048); SBAR()
        SBAR();
        if (KIND == 1) { hook(); SBAR(); }
        if (KIND == 2) { hook2(); SBAR(); }
        RD(0, 0);
        if (KIND == 0) { A_SETA(L, so2); A_A(L, FO, 0, 0); SBAR(); }
        RD(1, 0);
        if (KIND == 0) { A_A(L, FO, 1, 1024); SBAR(); }
        RD(2, 0);
        if (KIND == 0) { A_A(L, FO, 2, 2048); SBAR(); }
        RD(3, 0);
        if (KIND == 0) { A_A(L, FO, 3, 3072); SBAR(); }
        RD(0, 1);
        if (KIND == 0) { A_SETB(L, so2, wid); GL(L.vb[0], L.b, 0, ""); SBAR(); }
        RD(1, 1);
        if (KIND == 0) { GL(L.vb[1], L.b, 1024, ""); SBAR(); }
        RD(2, 1);
        if (KIND == 0 && !FO) { A_TOUCH(L); SBAR(); }
        RD(3, 1);
#undef RD
        int adv = so2 == STG_A ? -2 * (int)STG_A : (int)STG_A;
        so2 = so2 == 2 * STG_A ? 0 : so2 + STG_A;
        if (KIND == 0) { A_NEXTA(L, FO); A_NEXTB(L, FO); }
        asm volatile("" : "+s"(adv), "+s"(so2), "+s"(L.a), "+s"(L.b));
        if (!FO && KIND == 0) asm volatile("s_ashr_i32 %0, %1, 31\n\ts_add_i32 %1, %1, 1" : "=&s"(tm), "+s"(tc), "+s"(L.a3)::"scc");
        SBAR();
        if (KIND == 0) { if (FO) POST_BAR(6, 8, tc, L.post, tag, ws); else WAIT_BAR(8); } else if (KIND == 1) { __builtin_amdgcn_s_waitcnt(0x0070); BAR(); } else LG_BAR();
        if (KIND == 1 && !FO) PIN(L.scrap);
        SBAR();
#define ADV(i)                                                                                       \
    do {                                                                                             \
        if (KIND != 2) {                                                                             \
            if ((i) >= 18 && (i) < 20) { L.xb[(i) & 1] += adv; PIN(L.xb[(i) & 1]); }                 \
            if ((i) >= 20 && (i) < 20 + NP) { L.wb[(i) & 3] += adv; PIN(L.wb[(i) & 3]); }            \
            if ((i) >= 24 && (i) < 24 + NP && mode) { L.wb1[(i) & 3] += adv; PIN(L.wb1[(i) & 3]); }  \
        }                                                                                            \
        if (KIND == 0 && !FO) {                                                                      \
            if ((i) == 22) { L.tstep &= tm; PIN(L.tstep); }                                          \
            if ((i) == 23) { L.ta += L.tstep; PIN(L.ta); }                                           \
        }                                                                                            \
        SBAR();                                                                                      \
    } while (0)
        if (mode == 0) {
#pragma unroll
            for (int i = 0; i < 32; ++i) {
                if (i & 2) acc[i & 3][(i >> 2) & 3] = MFMA16(xf[(i >> 2) & 3][i >> 4], wf[i & 3][i >> 4], acc[i & 3][(i >> 2) & 3]);
                else acc[i & 3][(i >> 2) & 3] = MFMA16(wf[i & 3][i >> 4], xf[(i >> 2) & 3][i >> 4], acc[i & 3][(i >> 2) & 3]);
                ADV(i);
            }
        } else {
            const u32x4 u0 = __builtin_bit_cast(u32x4, wf[mode == 1 ? 2 : 0][0]), u1 = __builtin_bit_cast(u32x4, wf[mode == 1 ? 3 : 1][0]);
            float tmp = 0.f;
#pragma unroll
            for (int i = 0; i < 32; ++i) {
                const unsigned u = (i & 16) ? u1[(i >> 2) & 3] : u0[(i >> 2) & 3];
                if (i == 31) { cs3 += tmp; PIN(cs3); SBAR(); }
                acc[i & 3][(i >> 2) & 3] = MFMA16(wf[i & 3][i >> 4], xf[(i >> 2) & 3][i >> 4], acc[i & 3][(i >> 2) & 3]);
                if ((i & 1) == 0) { tmp = (i & 2) ? bfhi(u) : bflo(u); PIN(tmp); }
                else if (i == 31) {}
                else if (i & 16) { if (i & 2) { cs3 += tmp; PIN(cs3); } else { cs2 += tmp; PIN(cs2); } }
                else { if (i & 2) { cs1 += tmp; PIN(cs1); } else { cs0 += tmp; PIN(cs0); } }
                ADV(i);
            }
        }
#undef ADV
        if (KIND != 2) BAR();
        SBAR();
    };
#pragma unroll 1
    for (int t = 0; t < 14; ++t) body(IC<0>{});
    body(IC<1>{});
    body(IC<2>{});
    if (mode) {
        const float a = sum32(sum16(cs0 + cs1)), b = sum32(sum16(cs2 + cs3));
        LDS_AS float* csl = (LDS_AS float*)((lds_ptr)smem + CSL) + (wid >> 2) * 128 + (wid & 1) * 64 + (wid & 2) * 16 + (lane & 15);
        csl[0] = a; csl[16] = b;
    }
    if (wid < 4) LG_BAR();
}
template <int N>
DEV void read_cs(char* smem, int lane, int wid, f32x4 (&cs)[N]) {
    const LDS_AS float* csl = (const LDS_AS float*)((lds_ptr)smem + CSL) + (wid >> 2) * 128 + (lane >> 4) * 4;
#pragma unroll
    for (int nb = 0; nb < N; ++nb)
        cs[nb] = *(const LDS_AS f32x4*)(csl + (nb ^ (wid & 2)) * 16) + *(const LDS_AS f32x4*)(csl + 64 + (nb ^ (wid & 2)) * 16);
}

__global__ __launch_bounds__(512, 2) void k_mega(const u16* __restrict__ x, const u16* __restrict__ wqkv,
                                                 const u16* __restrict__ wo, const u16* __restrict__ w1,
                                                 const u16* __restrict__ w2, const u16* __restrict__ w3,
                                                 const u16* __restrict__ w4, const u16* __restrict__ gate_w,
                                                 char* __restrict__ ws, u16* __restrict__ out) {
    extern __shared__ __attribute__((aligned(16))) char smem[];
    const int b = blockIdx.x, xcd = b & 7, jb = b >> 3;
    const int wid = rfl(threadIdx.x >> 6);
    const unsigned lds0 = (unsigned)(uintptr_t)(lds_ptr)smem;
    const rsrc_t R = __builtin_amdgcn_make_buffer_rsrc(ws, 0, 0x7fffffff, 0x00020000);
    const int hp = 2 * xcd + (jb >> 4), rt = jb & 15;
    const int head = 2 * hp + (wid & 1), ks = wid >> 1, grp = wid >> 2;
    u32x2 tag;
    bf16x8 Q[4][2], Ka[2], Kb[2], Va[2], Vb[2], K0[4];
    u32x4 hv[4];
    unsigned fo1, ho;
    auto phase1 = [&](auto gc) {
        constexpr int G = decltype(gc)::value;
        const int lane = fresh_lane(), c = lane & 15, g = lane >> 4, wm = wid & 1, hh = (wid >> 1) & 1, h1 = 2 * hp + hh;
        LoadA L;
        setupA<false>(L, lane, wid, lds0, (const char*)(x + (size_t)rt * 128 * D_), (const char*)(wqkv + 64 * hp),
                      (const char*)(w1 + 64 * hp));
        u32x2 ep;
        {
            const unsigned z = 0;
            asm volatile("global_load_dwordx2 %0, %1, %2 sc1" : "=v"(ep) : "v"(z), "s"(ws) : "memory");
        }
        A_ALLB(L, false, 0, wid); A_ALLA(L, false, 0);
        const int srow = rt * 128 + hp * 8 + (wid & 3) * 2;
        const char* xr = (const char*)(x + (size_t)srow * D_);
        u32x4 gx[4];
        if (G)
            asm volatile("global_load_dwordx4 %0, %4, %5\n\tglobal_load_dwordx4 %1, %4, %5 offset:1024\n\t"
                         "global_load_dwordx4 %2, %4, %5 offset:2048\n\tglobal_load_dwordx4 %3, %4, %5 offset:3072"
                         : "=&v"(gx[0]), "=&v"(gx[1]), "=&v"(gx[2]), "=&v"(gx[3]) : "v"(lane * 16), "s"(xr) : "memory");
        A_ALLB(L, false, STG_A, wid); A_ALLA(L, false, STG_A);
        {
            const int j = (jb * 8 + wid) * 4 + (lane >> 4), m = (j >> 1) & 3;
            const unsigned col = (2 * xcd + (j & 1)) * 128;
            const bool w = jb < 16, w3rd = m < 3;
            L.tstep = !w ? 128 : w3rd ? 64 * 6144 : 64 * 2048;
            L.ta = (!w ? (u64)x : w3rd ? (u64)wqkv : (u64)w1) +
                   (!w ? ((xcd & 3) * 512 + j - 512) * 2048 + 384 : (192 + (j >> 3)) * (L.tstep >> 6) + (w3rd ? m * 2048 : 0) + col);
            L.scrap = 0;
            A_TOUCH(L);
            L.ta += L.tstep;
        }
        setupR<false>(L, smem, lane, wid);
        f32x4 acc[4][4] = {};
#pragma unroll
        for (int i = 0; i < 16; ++i) PIN(acc[i >> 2][i & 3]);
        if (G) asm volatile("s_waitcnt vmcnt(%5)\n\ts_barrier\n\ts_waitcnt vmcnt(%6)" : "+v"(ep), "+v"(gx[0]), "+v"(gx[1]), "+v"(gx[2]), "+v"(gx[3]) : "n"(11), "n"(7) : "memory");
        else asm volatile("s_waitcnt vmcnt(%1)\n\ts_barrier" : "+v"(ep) : "n"(7) : "memory");
        {
            const u64 e = ((u64)(unsigned)rfl(ep[1]) << 32 | (unsigned)rfl(ep[0])) + 1;
            tag = (u32x2){(unsigned)e, (unsigned)(e >> 32)};
        }
        const int rg = rt * 2 + wm;
        constexpr unsigned XQ = 32768;
        u32x4 rsv[4], gw[2];
        if (G) {
            const unsigned rso = WS_RS + (rg * 64 + c) * 16;
            float s1[2] = {0.f, 0.f}, s2[2] = {0.f, 0.f};
#pragma unroll
            for (int i = 0; i < 4; ++i)
#pragma unroll
                for (int e = 0; e < 4; ++e) {
                    const unsigned u = gx[i][e];
                    s1[i >> 1] = dot2(u, 0x3F803F80u, s1[i >> 1]);
                    s2[i >> 1] = dot2(u, u, s2[i >> 1]);
                }
            auto p1 = __builtin_amdgcn_permlane32_swap(__float_as_uint(s1[0]), __float_as_uint(s1[1]), false, false);
            auto p2 = __builtin_amdgcn_permlane32_swap(__float_as_uint(s2[0]), __float_as_uint(s2[1]), false, false);
            float a1 = __uint_as_float(p1[0]) + __uint_as_float(p1[1]), a2 = __uint_as_float(p2[0]) + __uint_as_float(p2[1]);
            a1 = dpp_add<0xB1>(a1); a2 = dpp_add<0xB1>(a2); a1 = dpp_add<0x4E>(a1); a2 = dpp_add<0x4E>(a2);
            a1 = dpp_add<0x141>(a1); a2 = dpp_add<0x141>(a2); a1 = dpp_add<0x140>(a1); a2 = dpp_add<0x140>(a2);
            a1 = sum16(a1); a2 = sum16(a2);
            const float mean = a1 * (1.0f / D_);
            const float rstd = __builtin_amdgcn_rsqf(__builtin_fmaxf(a2 * (1.0f / D_) - mean * mean, 0.f) + 1e-5f);
            stw2(R, (lane & 30) ? NOWHERE : (lane >> 5) * 16 + (lane & 1) * 8, WS_RS + srow * 16,
                 (u32x2){__float_as_uint((lane & 1) ? rstd : mean), tag[0]});
            BAR();
            kloopA<1>(ws, tag, smem, L, lane, wid, acc, [&]() {
#pragma unroll
                for (int mb = 0; mb < 4; ++mb) rsv[mb] = ldw4(R, rso + mb * 256, 0);
            }, []() {});
            const float sc = 0.17677669529663687f * 1.4426950408889634f;
            LDS_AS u32x4* qd = (LDS_AS u32x4*)((lds_ptr)smem + XQ + (wid & 3) * 4096 + (g >> 1) * 1024) + (g & 1) * 32 + c;
#pragma unroll
            for (int mb = 0; mb < 4; ++mb) qd[(mb >> 1) * 128 + (mb & 1) * 16] = pack44(acc[0][mb] * sc, acc[1][mb] * sc);
            RETRY(((rsv[0][1] & rsv[0][3] & rsv[1][1] & rsv[1][3] & rsv[2][1] & rsv[2][3] & rsv[3][1] & rsv[3][3]) ^ tag[0]) |
                      ((rsv[0][1] | rsv[0][3] | rsv[1][1] | rsv[1][3] | rsv[2][1] | rsv[2][3] | rsv[3][1] | rsv[3][3]) ^ tag[0]),
                  for (int mb = 0; mb < 4; ++mb) rsv[mb] = ldw4(R, rso + mb * 256, 0));
            fo1 = WS_FH + h1 * 8 + (lane == 0 ? (rg + 1) * 256 : rg == 0 ? 33 * 256 : 0);
            if (lane > (rg == 0 || rg == 31)) fo1 = NOWHERE;
        } else {
            kloopA<0>(ws, tag, smem, L, lane, wid, acc, [&]() {
#pragma unroll
                for (int i = 0; i < 4; ++i) gx[i] = *(const u32x4*)(xr + i * 1024 + lane * 16);
                gw[0] = *(const u32x4*)(gate_w + lane * 8);
                gw[1] = *(const u32x4*)(gate_w + 512 + lane * 8);
            }, []() {});
            const unsigned ko = (g >> 1) * 1024 + ((g & 1) * 32 + (c >> 3) * 16 + (c & 7)) * 16;
            const unsigned vo = 2048 + (g >> 1) * 1024 + ((g & 1) * 32 + c) * 16;
            const lds_ptr kd = (lds_ptr)smem + wid * 8192;
            const unsigned kvg = WS_KV + (h1 * 64 + 2 * rg) * 4096;
#pragma unroll
            for (int i = 0; i < 4; ++i) {
                const u32x4 kk = pack44(acc[0][i], acc[1][i]);
                const u32x4 vv = pack44(acc[2 + (i & 1)][i & 2], acc[2 + (i & 1)][(i & 2) + 1]);
                stw4(R, ko + (i & 1) * 128, kvg + (i >> 1) * 4096, kk);
                stw4(R, vo + (i & 1) * 256, kvg + (i >> 1) * 4096, vv);
                *(LDS_AS u32x4*)(kd + ko + (i >> 1) * 4096 + (i & 1) * 128) = kk;
                *(LDS_AS u32x4*)(kd + vo + (i >> 1) * 4096 + (i & 1) * 256) = vv;
            }
            *(LDS_AS unsigned*)(kd + PART + 7 * 1024 + lane * 16 + 12) = ~0u;
        }
        LG_BAR();
        {
            const lds_ptr kd = (lds_ptr)smem + (wid & 1) * 16384 + lane * 16, qd = (lds_ptr)smem + XQ + (wid & 1) * 8192 + lane * 16;
#pragma unroll
            for (int f = 0; f < 2; ++f) {
                K0[f] = ld128(kd + f * 1024); K0[2 + f] = ld128(qd + f * 1024);
#pragma unroll
                for (int i = 0; i < 4; ++i) Q[i][f] = ld128(qd + (i ^ ks ^ 3) * 2048 + f * 1024);
                Ka[f] = ld128(kd + ks * 4096 + f * 1024); Va[f] = ld128(kd + ks * 4096 + 2048 + f * 1024);
            }
        }
        f32x4 cs[2];
        if (G) read_cs(smem, lane, wid, cs);
        LG_BAR();
        if (G) {
            const int quarter = hp >> 2;
            const int sh = quarter == 0 ? 1 : quarter == 1 ? -1 : quarter == 2 ? 2 : -2;
#pragma unroll
            for (int mb = 0; mb < 4; ++mb) {
                const float mean = __uint_as_float(rsv[mb][0]), r = __uint_as_float(rsv[mb][2]);
                const float a = GC0 * r, bq = GC1 * r * r * r, k = __builtin_amdgcn_rcpf(r);
                const int t = rg * 64 + mb * 16 + c + sh, tw = t & (S_ - 1);
                hv[mb] = ln_gelu_chunk(acc[2][mb], acc[3][mb], cs[0], cs[1], mean, a, bq, k);
                if (mb == 0 || mb == 3) hv[mb] &= t == tw ? ~0u : 0u;
                if (mb == 0) ho = (tw >> 4) * 32768 + h1 * 1024 + (g * 16 + (tw & 15)) * 16;
                PIN(hv[mb]);
            }
        } else {
            post_flag(R, WS_F1 + (h1 * 32 + rg) * F1S, tag, lane);
            fo1 = ho = NOWHERE;
#pragma unroll
            for (int mb = 0; mb < 4; ++mb) hv[mb] = gx[mb];
            float z[2] = {0.f, 0.f};
#pragma unroll
            for (int i = 0; i < 4; ++i)
#pragma unroll
                for (int e = 0; e < 4; ++e) {
                    const unsigned u = gx[i][e], w = gw[i & 1][e];
                    z[i >> 1] = dot2(cvtpk(bflo(u) * bflo(w), bfhi(u) * bfhi(w)), 0x3F803F80u, z[i >> 1]);
                }
            auto p1 = __builtin_amdgcn_permlane32_swap(__float_as_uint(z[0]), __float_as_uint(z[1]), false, false);
            float a1 = __uint_as_float(p1[0]) + __uint_as_float(p1[1]);
            a1 = dpp_add<0xB1>(a1); a1 = dpp_add<0x4E>(a1); a1 = dpp_add<0x141>(a1); a1 = dpp_add<0x140>(a1);
            a1 = rbf(sum16(a1));
            const float gt = rbf(1.0f / (1.0f + __expf(-a1)));
            stw2(R, (lane & 31) ? NOWHERE : (lane >> 5) * 8, WS_G + srow * 8, (u32x2){cvtpk(gt, 1.0f - gt), tag[0]});
        }
    };
    if (grp) phase1(IC<1>{}); else phase1(IC<0>{});
    const bool p3_on = jb < 28;
    const int mt3 = 4 * (xcd & 1) + (jb & 3), nt3 = 7 * (xcd >> 1) + (jb >> 2);
    LoadA L3;
    f32x4 acc3[4][4];
    int kh, mt4, nt4;
    unsigned xv0, xv1, wva, wvb, dx4, dw4, f3p;
    const char *xs4, *wo4;
    auto setup4 = [&](int ln) {
        int bb = b;
        asm volatile("" : "+s"(bb));
        kh = ((bb >> 2) & 1) ^ 1; mt4 = 8 * (bb & 1) + ((bb >> 3) & 7); nt4 = 4 * ((bb >> 1) & 1) + ((bb >> 6) ^ 2);
        int wj = wid;
        asm volatile("" : "+s"(wj));
        PIN(ln);
        const int j4 = wj & 3, g4 = wj >> 2, m3 = 4 * (bb & 1) + ((bb >> 3) & 3), n3 = 7 * ((bb >> 1) & 1) + (bb >> 5);
        xv0 = (2 * (j4 ^ g4 ^ 1) + kh) * (144 * 1024) + ln * 16 + 2048;
        xv1 = (2 * (j4 ^ g4 ^ 1) + (kh ^ 1)) * (144 * 1024) + ln * 16;
        const int ir = 16 * j4 + (ln >> 3);
        wva = ir * 2048 + (((ln & 7) ^ (((ir >> 1) & 3) << 1)) << 4); wvb = wva + 8 * 2048 - 1024;
        xs4 = ws + WS_A4 + ((size_t)mt4 * 8 * 144 + kh * 72) * 1024 - 2048;
        wo4 = (const char*)(wo + (size_t)kh * 512 * D_ + nt4 * 128);
        dx4 = lds0 + j4 * 4096; dw4 = lds0 + 16384 + j4 * 2048;
        f3p = WS_F3 + (((m3 * 2 + (j4 >> 1)) * 2 + (kh ^ 1)) * 64 + g4 * 28 + n3 * 2 + (j4 & 1)) * 8;
    };
    u32x4 st[4];
    u32x2 og;
    int srow;
    auto rowc = [&]() {
        RETRY(og[1] ^ tag[0], og = ldw2(R, srow * 8, WS_G + mt3 * 2048));
        const f32x4 sv = (__builtin_bit_cast(f32x4, st[0]) + __builtin_bit_cast(f32x4, st[1])) +
                         (__builtin_bit_cast(f32x4, st[2]) + __builtin_bit_cast(f32x4, st[3]));
        auto p1 = __builtin_amdgcn_permlane32_swap(__float_as_uint(sv[0]), __float_as_uint(sv[2]), false, false);
        auto p2 = __builtin_amdgcn_permlane32_swap(__float_as_uint(sv[1]), __float_as_uint(sv[3]), false, false);
        const float mean = sum16(__uint_as_float(p1[0]) + __uint_as_float(p1[1])) * (1.0f / D_);
        const float var = sum16(__uint_as_float(p2[0]) + __uint_as_float(p2[1])) * (1.0f / D_) - mean * mean;
        const float r = __builtin_amdgcn_rsqf(__builtin_fmaxf(var, 0.f) + 1e-5f);
        st[0] = __builtin_bit_cast(u32x4, (f32x4){mean, GC0 * r, GC1 * r * r * r, __builtin_fminf(__builtin_amdgcn_rcpf(r * bfhi(og[0])), 1e30f)});
        PIN(st[0]);
    };
    {
        int lane = fresh_lane();
        const unsigned kvh = WS_KV + head * 64 * 4096;
        unsigned vl = lane * 16;
        int kt = rt >> 1;
#define KVO(t) (kvh + ((t) * 8 + 2 * ks) * 4096)
#define LDF(so, imm) BC8(ldw4(R, vl + (imm), so))
        float mref = -INFINITY;
        {
            f32x16 s = MFMA32(K0[0], K0[2], (f32x16){});
            s = MFMA32(K0[1], K0[3], s);
#pragma unroll
            for (int r = 0; r < 16; r += 2) mref = __builtin_fmaxf(__builtin_fmaxf(s[r], s[r + 1]), mref);
        }
        mref = max32(mref);
        f32x16 O[4] = {};
        f32x4 Ls = {};
#define EX(d, X, i) asm volatile("v_exp_f32 %0, %1" : "=v"(d) : "v"(X[i]))
#define CV(P, k, a, b) do { unsigned t_; asm volatile("v_cvt_pk_bf16_f32 %0, %1, %2" : "=v"(t_) : "v"(a), "v"(b)); P[(k) >> 2][(k) & 3] = t_; } while (0)
#define MM32(D, A, B) do { SBAR(); D = MFMA32(BC8(A), BC8(B), D); PIN(D); SBAR(); } while (0)
#define MM16(D, A, B) asm volatile("v_mfma_f32_16x16x32_bf16 %0, %1, %2, %0" : "+v"(D) : "v"(A), "v"(B))
#define STEP(Sc, Sn, Pc, Pp, Kn, qn, Vp, bp) STEPX(true, Sc, Sn, Pc, Pp, Kn, qn, Vp, bp)
#define STEPX(NEXT, Sc, Sn, Pc, Pp, Kn, qn, Vp, bp)                                                             \
    do {                                                                                                        \
        float a0, a1, a2, a3, b0, b1, b2, b3;                                                                   \
        SBAR();                                                                                                 \
        if (NEXT) { Sn = MFMA32(Kn[0], Q[qn][0], C); PIN(Sn); SBAR(); }                                         \
        EX(a0, Sc, 0); EX(a1, Sc, 1); EX(a2, Sc, 2); EX(a3, Sc, 3); CV(Pp, 6, E[0], E[1]); CV(Pp, 7, E[2], E[3]); \
        MM32(O[bp], Vp[0], Pp[0]);                                                                              \
        EX(b0, Sc, 4); EX(b1, Sc, 5); EX(b2, Sc, 6);                                                            \
        MM16(Ls, sel[bp], Pp[0]);                                                                               \
        EX(b3, Sc, 7); CV(Pc, 0, a0, a1); CV(Pc, 1, a2, a3);                                                    \
        if (NEXT) MM32(Sn, Kn[1], Q[qn][1]);                                                                    \
        EX(a0, Sc, 8); EX(a1, Sc, 9); EX(a2, Sc, 10); EX(a3, Sc, 11); CV(Pc, 2, b0, b1); CV(Pc, 3, b2, b3);     \
        MM32(O[bp], Vp[1], Pp[1]);                                                                              \
        EX(E[0], Sc, 12); EX(E[1], Sc, 13); EX(E[2], Sc, 14);                                                   \
        MM16(Ls, sel[bp], Pp[1]);                                                                               \
        EX(E[3], Sc, 15); CV(Pc, 4, a0, a1); CV(Pc, 5, a2, a3);                                                 \
        SBAR();                                                                                                 \
    } while (0)
        unsigned scrap = 0;
        const char* t3 = (const char*)w2 + (b * 8 + wid) * 1024;
#define TCH3() TCH(1 << 20)
#define ATT_INIT()                                                                                              \
        bf16x8 sel[4];                                                                                          \
        _Pragma("unroll") for (int i = 0; i < 4; ++i) {                                                         \
            const short v = ((lane & 3) == i && ((lane >> 2) & 1) == ((lane >> 4) & 1)) ? (short)0x3F80 : (short)0; \
            sel[i] = (bf16x8){v, v, v, v, v, v, v, v};                                                          \
            PIN(sel[i]);                                                                                        \
        }                                                                                                       \
        f32x16 C, SA, SB;                                                                                       \
        PIN(mref);                                                                                              \
        _Pragma("unroll") for (int r = 0; r < 16; ++r) C[r] = -mref;                                            \
        PIN(C);                                                                                                 \
        u32x4 PA[2] = {}, PB[2] = {};                                                                           \
        float E[4] = {};                                                                                        \
        SA = MFMA32(Ka[0], Q[0][0], C);                                                                         \
        SA = MFMA32(Ka[1], Q[0][1], SA);                                                                        \
        asm volatile("s_nop 7\n\ts_nop 7" : "+v"(SA), "+v"(PB[0]), "+v"(PB[1]), "+v"(Vb[0]), "+v"(Vb[1]), "+v"(O[0]),     \
                     "+v"(O[1]), "+v"(O[2]), "+v"(O[3]), "+v"(Ls))
#define ATT_FLAG(fl, fa) if (__builtin_expect(any(diff(fl, tag)), 0)) wait_flags(R, fa, tag)
#define ATT_RUN(nit, TOP, MID, POST)                                                                                \
        _Pragma("unroll 1") for (int it = 0; it < (nit); ++it) {                                                \
            const unsigned kvo = KVO(kt);                                                                       \
            kt = (kt + 1) & 7;                                                                                  \
            const unsigned n2 = KVO(kt), fa = WS_F1 + (head * 32 + kt * 4 + ks) * F1S;                            \
            TOP;                                                                                                \
            const u32x2 fl = ldw2(R, 0, fa);                                                                    \
            STEP(SA, SB, PA, PB, Ka, 1, Vb, 3);                                                                 \
            Vb[0] = LDF(kvo + 4096, 2048); Vb[1] = LDF(kvo + 4096, 3072);                                       \
            TCH3();                                                                                             \
            STEP(SB, SA, PB, PA, Ka, 2, Va, 0);                                                                 \
            MID;                                                                                                \
            STEP(SA, SB, PA, PB, Ka, 3, Va, 1);                                                                 \
            ATT_FLAG(fl, fa);                                                                                   \
            POST;                                                                                               \
            Ka[0] = LDF(n2, 0); Ka[1] = LDF(n2, 1024);                                                          \
            STEP(SB, SA, PB, PA, Kb, 0, Va, 2);                                                                 \
            MID;                                                                                                \
            STEP(SA, SB, PA, PB, Kb, 1, Va, 3);                                                                 \
            Va[0] = LDF(n2, 2048); Va[1] = LDF(n2, 3072);                                                       \
            STEP(SB, SA, PB, PA, Kb, 2, Vb, 0);                                                                 \
            MID;                                                                                                \
            STEP(SA, SB, PA, PB, Kb, 3, Vb, 1);                                                                 \
            Kb[0] = LDF(n2 + 4096, 0); Kb[1] = LDF(n2 + 4096, 1024);                                            \
            STEP(SB, SA, PB, PA, Ka, 0, Vb, 2);                                                                 \
        }
#define ATT_END()                                                                                               \
        CV(PB, 6, E[0], E[1]); CV(PB, 7, E[2], E[3]);                                                           \
        MM32(O[3], Vb[0], PB[0]); MM16(Ls, sel[3], PB[0]); MM32(O[3], Vb[1], PB[1]); MM16(Ls, sel[3], PB[1]);   \
        asm volatile("s_nop 7\n\ts_nop 7\n\ts_nop 3\n\ts_waitcnt lgkmcnt(0)"                                     \
                     : "+v"(O[0]), "+v"(O[1]), "+v"(O[2]), "+v"(O[3]), "+v"(Ls), "+s"(scrap))
        const int mt2 = 4 * (xcd >> 1) + (jb & 3), nt2 = 8 * (xcd & 1) + (jb >> 2);
        const int n1 = grp ? N1B : N1A;
        {
            const int ir = lane >> 3;
            const unsigned wv = ir * 2048 + (((lane & 7) ^ (((ir >> 1) & 3) << 1)) << 4);
            const char* wp = (const char*)(w2 + (size_t)(128 * wid) * D_ + nt2 * 64);
            unsigned wd = lds0 + wid * 16384;
#define W2L() do { GLDS(wd, wv, wp, 0, ""); wd += 1024; wp += 16384; } while (0)
            if (grp) __builtin_amdgcn_s_setprio(2); else __builtin_amdgcn_s_setprio(1);
            TCH(512);
            kt = (kt + 1) & 7;
            const unsigned fa = WS_F1 + (head * 32 + (lane < 32 ? (rt ^ 1) * 2 + (ks >> 1) : kt * 4 + ks)) * F1S;
            const u32x2 f0 = ldw2(R, fa, 0);
            Vb[0] = Vb[1] = (bf16x8){};
            ATT_INIT();
            {
                const unsigned nb = kvh + ((rt ^ 1) * 4 + ks) * 4096, n2 = KVO(kt);
                STEP(SA, SB, PA, PB, Ka, 1, Vb, 3);
                TCH(0);
                t3 = (const char*)w3 + (b * 8 + wid) * 512;
                ATT_FLAG(f0, fa);
                Kb[0] = LDF(nb, 0); Kb[1] = LDF(nb, 1024);
                Vb[0] = LDF(nb, 2048); Vb[1] = LDF(nb, 3072);
#pragma unroll
                for (int mb = 0; mb < 4; ++mb) stw4(R, (ho + mb * 32768) & (NOWHERE | (4 * MB - 1)), WS_HS, hv[mb]);
                STEP(SB, SA, PB, PA, Ka, 2, Va, 0);
                STEP(SA, SB, PA, PB, Ka, 3, Va, 1);
                Ka[0] = LDF(n2, 0); Ka[1] = LDF(n2, 1024);
                STEP(SB, SA, PB, PA, Kb, 0, Va, 2);
                TCH3();
                STEP(SA, SB, PA, PB, Kb, 1, Va, 3);
                Va[0] = LDF(n2, 2048); Va[1] = LDF(n2, 3072);
                STEP(SB, SA, PB, PA, Kb, 2, Vb, 0);
                W2L(); W2L();
                STEP(SA, SB, PA, PB, Kb, 3, Vb, 1);
                Kb[0] = LDF(n2 + 4096, 0); Kb[1] = LDF(n2 + 4096, 1024);
                STEP(SB, SA, PB, PA, Ka, 0, Vb, 2);
                asm volatile("s_waitcnt vmcnt(8)" ::: "memory");
                stw2(R, fo1, 0, tag);
                W2L(); W2L();
            }
            ATT_RUN(n1 - 1, if (it == N1A - 1) asm volatile("s_waitcnt vmcnt(2)\n\ts_barrier" ::: "memory"),
                    if (it == 0) { W2L(); W2L(); W2L(); W2L(); }, );
            PIN(SA);
            ATT_END();
            PIN(SA);
            __builtin_amdgcn_s_setprio(0);
            if (!grp) { DRAIN(); BAR(); }
        }
        {
            lane = fresh_lane();
            vl = lane * 16;
            const int c = lane & 15, g = lane >> 4, q = c >> 2, mb = mt2 * 8 + (wid & 3) * 2;
#pragma unroll
            for (int i = 0; i < 2; ++i) stw4(R, vl + i * 1024, WS_EX + b * 16384 + wid * 2048, (u32x4){~0u, ~0u, ~0u, ~0u});
            wait_flags(R, WS_FH + (((mb * 16 - 2) >> 6) + 1) * 256 + lane * 8, tag);
            const unsigned hso = WS_HS + mb * 32768 + grp * 16384;
            bf16x8 X[8][2];
#pragma unroll
            for (int i = 0; i < 16; ++i) X[i >> 1][i & 1] = LDF(hso + (i & 1) * 32768 + (i >> 3) * 4096, ((i >> 1) & 3) * 1024);
            lds_ptr wb[4];
#pragma unroll
            for (int nb = 0; nb < 4; ++nb)
                wb[nb] = (lds_ptr)smem + grp * 65536 + (4 * g + q) * 128 + ((nb ^ ((2 * g + (q >> 1)) & 3)) << 5) + (lane & 3) * 8;
            LDS_AS f32x4* part = (LDS_AS f32x4*)((lds_ptr)smem + PART + (wid & 3) * 8192) + lane;
            f32x4 a2[2][4] = {};
            bf16x8 W[2][4];
            u32x4 xr[2][2];
#pragma unroll
            for (int nb = 0; nb < 4; ++nb) W[0][nb] = ldtr(wb[nb]);
#pragma unroll
            for (int kb = 0; kb < 16; ++kb) {
                if (kb < 15)
#pragma unroll
                    for (int nb = 0; nb < 4; ++nb) W[(kb + 1) & 1][nb] = ldtr(wb[nb] + (kb + 1) * 4096);
                SBAR();
#pragma unroll
                for (int i = 0; i < 8; ++i) a2[i & 1][i >> 1] = MFMA16(W[kb & 1][i >> 1], X[kb & 7][i & 1], a2[i & 1][i >> 1]);
                SBAR();
                if (kb < 8)
#pragma unroll
                    for (int m = 0; m < 2; ++m) X[kb][m] = LDF(hso + m * 32768 + ((kb + 8) >> 2) * 4096, (kb & 3) * 1024);
                if (kb == 9)
#pragma unroll
                    for (int i = 0; i < 4; ++i)
                        xr[i >> 1][i & 1] = *(const u32x4*)(x + (size_t)((mb + (i >> 1)) * 16 + c) * D_ + nt2 * 64 + (i & 1) * 32 + (g & 1) * 16 + (g >> 1) * 8);
                if (kb == 11) { Ka[0] = LDF(KVO(kt), 0); Ka[1] = LDF(KVO(kt), 1024); Va[0] = LDF(KVO(kt), 2048); Va[1] = LDF(KVO(kt), 3072); }
                if (kb == 13) { Kb[0] = LDF(KVO(kt) + 4096, 0); Kb[1] = LDF(KVO(kt) + 4096, 1024); }
            }
            __builtin_amdgcn_s_setprio(3);
            if (grp) {
#pragma unroll 1
                for (unsigned n = 0; n < SPIN_CAP; ++n) {
                    if (!any(((volatile LDS_AS unsigned*)(part + 7 * 64))[3] == ~0u)) break;
                    __builtin_amdgcn_s_sleep(2);
                }
                asm volatile("" ::: "memory");
#pragma unroll
                for (int i = 0; i < 8; ++i) a2[i >> 2][i & 3] += part[i * 64];
#pragma unroll
                for (int m = 0; m < 2; ++m) {
                    float s1 = 0.f, s2 = 0.f;
#pragma unroll
                    for (int p = 0; p < 2; ++p) {
                        auto u = __builtin_amdgcn_permlane16_swap(xr[m][p][0], xr[m][p][2], false, false);
                        auto v = __builtin_amdgcn_permlane16_swap(xr[m][p][1], xr[m][p][3], false, false);
                        const f32x4 lo = {bflo(u[0]), bfhi(u[0]), bflo(v[0]), bfhi(v[0])}, hi = {bflo(u[1]), bfhi(u[1]), bflo(v[1]), bfhi(v[1])};
                        const u32x4 o = pack44(a2[m][2 * p] + lo, a2[m][2 * p + 1] + hi);
#pragma unroll
                        for (int e = 0; e < 4; ++e) { const unsigned w = o[e]; s1 = dot2(w, 0x3F803F80u, s1); s2 = dot2(w, w, s2); }
                        stw4(R, vl, WS_X1 + ((mb + m) * 32 + nt2 * 2 + p) * 1024, o);
                    }
                    s1 = sum32(sum16(s1)); s2 = sum32(sum16(s2));
                    stw2(R, g ? NOWHERE : c * 8, WS_ST + ((mb + m) * 16 + nt2) * 128, (u32x2){__float_as_uint(s1), __float_as_uint(s2)});
                }
            } else {
#pragma unroll
                for (int i = 0; i < 7; ++i) part[i * 64] = a2[i >> 2][i & 3];
                asm volatile("" ::: "memory");
                a2[1][3][3] = __uint_as_float(min(__float_as_uint(a2[1][3][3]), ~1u));
                part[7 * 64] = a2[1][3];
            }
            fo1 = lane == 0 ? WS_FX + (b * 8 + wid) * 8
                            : lane > grp ? NOWHERE : WS_F2 + (mt2 >> 1) * 2048 + (((mt2 & 1) * 16 + nt2) * 4 + (wid & 3)) * 8;
            if (!grp) __builtin_amdgcn_s_setprio(1);
        }
        setupA<true>(L3, lane, wid, lds0, ws + WS_X1 + (size_t)mt3 * 16 * 32768, (const char*)(w3 + nt3 * 128), nullptr);
        const int blk = rt * 4 + ks;
        u32x2 gv;
        constexpr unsigned CMB = 96 * 1024;
#define OPUT(d, i) _Pragma("unroll") for (int e = 0; e < 4; ++e) (d)[e * 64] = (f32x4){O[i][4 * e], O[i][4 * e + 1], O[i][4 * e + 2], O[i][4 * e + 3]}
        LDS_AS f32x4* me;
        {
            Vb[0] = Vb[1] = (bf16x8){};
            ATT_INIT();
            ATT_RUN(7 - n1, , , if (it == 0) {
                asm volatile("s_waitcnt vmcnt(2)" ::: "memory");
                stw2(R, fo1, 0, tag);
            });
            const unsigned kvo = KVO(kt), f2o = WS_F2 + mt3 * 2048;
            BAR();
            if (grp) __builtin_amdgcn_s_setprio(0);
            STEP(SA, SB, PA, PB, Ka, 1, Vb, 3);
            Vb[0] = LDF(kvo + 4096, 2048); Vb[1] = LDF(kvo + 4096, 3072);
            gv = ldw2(R, (lane & 31) * 8, WS_G + blk * 256);
            u32x4 f2 = ldw4(R, vl, f2o);
            STEP(SB, SA, PB, PA, Ka, 2, Va, 0);
            STEP(SA, SB, PA, PB, Ka, 3, Va, 1);
            RETRY((f2[0] ^ tag[0]) | (f2[1] ^ tag[1]) | (f2[2] ^ tag[0]) | (f2[3] ^ tag[1]) | (gv[1] ^ tag[0]),
                  gv = ldw2(R, (lane & 31) * 8, WS_G + blk * 256); f2 = ldw4(R, vl, f2o));
            if (p3_on) { A_ALLB(L3, true, 0, wid); A_ALLA(L3, true, 0); }
            STEP(SB, SA, PB, PA, Kb, 0, Va, 2);
            if (p3_on) { A_ALLB(L3, true, STG_A, wid); A_ALLA(L3, true, STG_A); }
            STEP(SA, SB, PA, PB, Kb, 1, Va, 3);
            STEP(SB, SA, PB, PA, Kb, 2, Vb, 0);
            STEP(SA, SB, PA, PB, Kb, 3, Vb, 1);
            me = (LDS_AS f32x4*)((lds_ptr)smem + CMB + wid * 8192 + vl);
            OPUT(me, 0);
            STEPX(false, SB, SA, PB, PA, Ka, 0, Vb, 2);
            OPUT(me + 256, 1);
            SBAR();
            ATT_END();
            __builtin_amdgcn_s_setprio(0);
        }
#undef ATT_RUN
#undef ATT_END
#undef ATT_INIT
#undef ATT_FLAG
#undef W2L
#undef TCH3
#undef STEP
#undef STEPX
#undef MM32
#undef MM16
        f32x16 o;
        float l;
        {
            srow = (wid * 2 + ((lane >> 3) & 1)) * 16 + (lane & 7) * 2 + (lane >> 5);
#pragma unroll
            for (int i = 0; i < 4; ++i)
                st[i] = ldw4(R, ((lane >> 3) & 1) * 2048 + (lane >> 4) * 512 + (lane & 7) * 16 + i * 128, WS_ST + (mt3 * 16 + wid * 2) * 2048);
            og = ldw2(R, srow * 8, WS_G + mt3 * 2048);
            if (!grp) {
                setup4(lane);
                asm volatile("" : "+s"(xs4), "+s"(wo4), "+s"(dx4), "+s"(dw4), "+s"(kh), "+s"(mt4), "+s"(nt4));
                PIN(xv0); PIN(xv1); PIN(wva); PIN(wvb); PIN(f3p);
                rowc();
            }
            LG_BAR();
            LDS_AS f32x4* pa = (LDS_AS f32x4*)((lds_ptr)smem + CMB + (wid ^ 4) * 8192) + lane;
#pragma unroll
            for (int i = 0; i < 2; ++i)
#pragma unroll
                for (int e = 0; e < 4; ++e) {
                    const f32x4 v = pa[(i * 4 + e) * 64];
#pragma unroll
                    for (int j = 0; j < 4; ++j) O[2 + i][4 * e + j] += v[j];
                }
            OPUT(pa, 2);
            pa[256] = Ls;
            SBAR();
            setupR<true>(L3, smem, lane, wid);
#pragma unroll
            for (int i = 0; i < 16; ++i) { acc3[i >> 2][i & 3] = (f32x4){}; PIN(acc3[i >> 2][i & 3]); }
            L3.post = WS_FO + (((blk >> 2) * 2 + (head >> 4)) * 64 + (head & 15) * 4 + (blk & 3)) * 8;
            PIN(L3.post);
            LG_BAR();
            const LDS_AS f32x4* p6 = (const LDS_AS f32x4*)((lds_ptr)smem + CMB + (wid ^ 6) * 8192) + lane;
            l = (Ls[3] + ((const LDS_AS float*)(p6 + 256))[2]) +
                (((const LDS_AS float*)(me + 256))[1] + ((const LDS_AS float*)((lds_ptr)smem + CMB + (wid ^ 2) * 8192 + 4096 + vl))[0]);
#pragma unroll
            for (int e = 0; e < 4; ++e) {
                const f32x4 v = p6[e * 64];
#pragma unroll
                for (int j = 0; j < 4; ++j) o[4 * e + j] = O[3][4 * e + j] + v[j];
            }
        }
#undef OPUT
        {
            float n = 0.f;
#pragma unroll
            for (int r = 0; r < 16; ++r) n = __builtin_fmaf(o[r], 0.f, n);
            const bool ok = l >= 0x1p-50f && l <= 0x1p50f && n == 0.f;
            if (__builtin_expect(!__all(ok), 0)) {
                const bf16x8 q0 = Q[3][0], q1 = Q[3][1];
                wait_flags(R, WS_F1 + (head * 32 + (lane & 31)) * F1S, tag);
                float m = -INFINITY;
#pragma unroll 1
                for (int t = 0; t < 64; ++t) {
                    f32x16 s = MFMA32(LDF(kvh + t * 4096, 0), q0, (f32x16){});
                    s = MFMA32(LDF(kvh + t * 4096, 1024), q1, s);
#pragma unroll
                    for (int r = 0; r < 16; ++r) m = __builtin_fmaxf(m, s[r]);
                }
                m = max32(m);
                o = (f32x16){};
                l = 0.f;
#pragma unroll 1
                for (int t = 0; t < 64; ++t) {
                    f32x16 s = MFMA32(LDF(kvh + t * 4096, 0), q0, (f32x16){});
                    s = MFMA32(LDF(kvh + t * 4096, 1024), q1, s);
                    u32x4 p[2];
#pragma unroll
                    for (int r = 0; r < 8; ++r) {
                        const unsigned w = cvtpk(__builtin_amdgcn_exp2f(s[2 * r] - m), __builtin_amdgcn_exp2f(s[2 * r + 1] - m));
                        p[r >> 2][r & 3] = w;
                        l += bflo(w) + bfhi(w);
                    }
                    o = MFMA32(LDF(kvh + t * 4096, 2048), BC8(p[0]), o);
                    o = MFMA32(LDF(kvh + t * 4096, 3072), BC8(p[1]), o);
                }
                l = sum32(l);
            }
        }
        {
            const float sc = bflo(gv[0]) / l;
            const unsigned vo = ((lane >> 4) & 1) * (144 * 1024) + ((lane >> 5) * 16 + (lane & 15)) * 16;
#pragma unroll
            for (int u = 0; u < 2; ++u)
                stw4(R, vo + u * 512, WS_A4 + (blk * 2 * 144 + (head >> 4) * 72 + (head & 15)) * 1024,
                     pack44((f32x4){o[4 * u], o[4 * u + 1], o[4 * u + 2], o[4 * u + 3]} * sc,
                            (f32x4){o[8 + 4 * u], o[9 + 4 * u], o[10 + 4 * u], o[11 + 4 * u]} * sc));
        }
        __builtin_amdgcn_s_waitcnt(0x0F72);
#undef LDF
#undef KVO
#undef EX
#undef CV
    }
    {
        const int lane = fresh_lane(), c = lane & 15, g = lane >> 4, q = c >> 2;
        const int j = wid & 3, wm = j & 1, wn = j >> 1;
        constexpr unsigned STG_B = 32768;
#define B_SET(tile)                                                                          \
    const int tl_ = (tile);                                                                  \
    const char* bx_ = xs4 + tl_ * 2048;                                                      \
    const char* bw_ = wo4 + tl_ * 131072;                                                    \
    const char* bw1_ = bw_ + 128;                                                            \
    const unsigned bo_ = (tl_ & 3) * STG_B
#define B_X0() GLDS(dx4 + bo_, xv0, bx_, 0, " sc1")
#define B_X1() GLDS(dx4 + bo_, xv0, bx_, 1024, " sc1")
#define B_X2() GLDS(dx4 + bo_, xv1, bx_, 2048, " sc1")
#define B_X3() GLDS(dx4 + bo_, xv1, bx_, 3072, " sc1")
#define B_W0() GLDS(dw4 + bo_, wva, bw_, 0, "")
#define B_W1() GLDS(dw4 + bo_, wvb, bw_, 1024, "")
#define B_W2() GLDS(dw4 + 8192 + bo_, wva, bw1_, 0, "")
#define B_W3() GLDS(dw4 + 8192 + bo_, wvb, bw1_, 1024, "")
#define B_ALL(tile) do { B_SET(tile); B_W0(); B_W1(); B_W2(); B_W3(); B_X0(); B_X1(); B_X2(); B_X3(); } while (0)
        const unsigned foo = WS_FO + ((8 * (xcd & 1) + (jb & 7)) * 2 + ((xcd >> 2) ^ 1)) * 512 + lane * 8;
        if (p3_on) {
            f32x4 (&acc)[4][4] = acc3;
            WAIT_BAR(8);
            if (grp) rowc();
            *(LDS_AS u32x4*)((lds_ptr)smem + RCL + grp * 8192 + (srow & 127) * 16) = st[0];
            if (grp) BAR();
            u32x2 fo;
            kloopA<2>(ws, tag, smem, L3, lane, wid, acc, []() {}, [&]() { fo = ldw2(R, foo, 0); });
            if (grp) { setup4(lane); LG_BAR(); }
            if (any(diff(fo, tag))) wait_flags(R, foo, tag);
            f32x4 cs[4];
            read_cs(smem, lane, wid, cs);
            const unsigned a4o = WS_A4 + ((mt3 * 16 + j * 4) * 144 + (nt3 / 14) * 72 + 16 + grp * 28 + (nt3 % 14) * 2) * 1024;
#pragma unroll
            for (int mb = 0; mb < 4; ++mb) {
                const f32x4 rc = *(const LDS_AS f32x4*)((lds_ptr)smem + RCL + (j >> 1) * 8192 + ((j & 1) * 64 + mb * 16 + c) * 16);
#pragma unroll
                for (int p = 0; p < 2; ++p) {
                    if (mb < 2) {
                        B_SET(2 * mb + 1 - grp);
                        if (p) { B_X0(); B_X1(); B_X2(); B_X3(); } else { B_W0(); B_W1(); B_W2(); B_W3(); }
                        SBAR();
                    }
                    stw4(R, lane * 16, a4o + (mb * 144 + (p ^ (j >> 1))) * 1024,
                         ln_gelu_chunk(acc[2 * p][mb], acc[2 * p + 1][mb], cs[2 * p], cs[2 * p + 1], rc[0], rc[1], rc[2], rc[3]));
                }
                if (mb == 0) { SBAR(); if (!grp) BAR(); SBAR(); }
            }
            if (!grp) post_flag(R, f3p, tag, lane);
        } else {
            DRAIN();
            stw2(R, lane == 0 ? L3.post : NOWHERE, 0, tag);
            if (grp) setup4(lane);
            const int idx = (xcd * 4 + jb - 28) * 8 + wid;
            unsigned scrap = 0;
            asm volatile("global_load_dword %0, %1, %2" : "+v"(scrap) : "v"((idx * 64 + lane) * 128), "s"(wo) : "memory");
#pragma unroll 1
            for (int i = 0; i < 4; ++i) {
                __builtin_amdgcn_s_sleep(24);
                const unsigned vo = ((((i & 1) * 2 + (i >> 1)) * 256 + idx) * 56 + (lane < 56 ? lane : 55)) * 128;
                asm volatile("global_load_dword %0, %1, %2" : "+v"(scrap) : "v"(vo), "s"(w4) : "memory");
            }
            asm volatile("s_waitcnt vmcnt(0)" : "+v"(scrap)::"memory");
            wait_flags(R, foo, tag);
            B_ALL(1 - grp); B_ALL(3 - grp);
            DRAIN();
        }
        WAIT_BAR(5);
        if (grp) BAR();
        lds_ptr xb = (lds_ptr)smem + grp * STG_B + (8 * wm) * 1024 + lane * 16, wb[4];
#pragma unroll
        for (int nb = 0; nb < 4; ++nb)
            wb[nb] = (lds_ptr)smem + grp * STG_B + 16384 + wn * 8192 + (4 * g + q) * 128 + ((nb ^ ((2 * g + (q >> 1)) & 3)) << 5) + (lane & 3) * 8;
        f32x4 acc[4][4] = {};
        const int pb = b ^ 4;
        const char* w44 = (const char*)(w4 + (size_t)(kh * 1792 + (1 - grp) * 128) * D_ + nt4 * 128);
        unsigned f3o = lane < 28 ? WS_F3 + (mt4 * 2 + kh) * 512 + lane * 8 : WS_FX + (pb * 8 + wid) * 8;
        const int mblk = mt4 * 8 + wm * 4 + grp * 2 + kh;
        const unsigned go = WS_G + mblk * 128 + c * 8;
        const char *bx = xs4 + (5 - grp) * 2048, *bw = wo4 + (5 - grp) * 131072;
        unsigned bo = (1 - grp) * STG_B, bstep = 2 * 131072;
        int adv = 2 * STG_B, tc = grp && p3_on ? 0 : 2;
        PIN(tag); PIN(f3p);
        u32x2 f3 = {0u, 0u};
        auto body = [&](auto kind) {
            constexpr int KIND = decltype(kind)::value;
            bf16x8 xf[4][2], wf[4][2];
            const char* bw1 = bw + 128;
            SBAR();
            if (KIND == 0) asm volatile("global_load_dwordx2 %0, %1, %2 sc1" : "+v"(f3) : "v"(f3o), "s"(ws) : "memory");
            wf[0][0] = ldtr(wb[0]); xf[0][0] = ld128(xb); SBAR();
            if (KIND == 0) { M0SET(dx4 + bo); GL(xv0, bx, 0, " sc1"); SBAR(); }
            wf[1][0] = ldtr(wb[1]); xf[1][0] = ld128(xb + 2048); SBAR();
            if (KIND == 0) { GL(xv0, bx, 1024, " sc1"); SBAR(); }
            wf[2][0] = ldtr(wb[2]); xf[2][0] = ld128(xb + 4096); SBAR();
            if (KIND == 0) { GL(xv1, bx, 2048, " sc1"); SBAR(); }
            wf[3][0] = ldtr(wb[3]); xf[3][0] = ld128(xb + 6144); SBAR();
            if (KIND == 0) { GL(xv1, bx, 3072, " sc1"); SBAR(); }
            wf[0][1] = ldtr(wb[0] + 4096); xf[0][1] = ld128(xb + 1024); SBAR();
            if (KIND == 0) { M0SET(dw4 + bo); GL(wva, bw, 0, ""); SBAR(); }
            wf[1][1] = ldtr(wb[1] + 4096); xf[1][1] = ld128(xb + 3072); SBAR();
            if (KIND == 0) { GL(wvb, bw, 1024, ""); SBAR(); }
            wf[2][1] = ldtr(wb[2] + 4096); xf[2][1] = ld128(xb + 5120); SBAR();
            if (KIND == 0) { M0SET(dw4 + 8192 + bo); GL(wva, bw1, 0, ""); SBAR(); }
            wf[3][1] = ldtr(wb[3] + 4096); xf[3][1] = ld128(xb + 7168); SBAR();
            if (KIND == 0) {
                GL(wvb, bw1, 1024, "");
                bx += 4096; bw += bstep; bo ^= 2 * STG_B;
                asm volatile("" : "+s"(bx), "+s"(bw), "+s"(bo));
                SBAR();
                POST_BAR(9, 14, tc, f3p, tag, ws);
            } else {
                WAIT_BAR(0);
            }
            SBAR();
#pragma unroll
            for (int i = 0; i < 32; ++i) {
                acc[i & 3][(i >> 2) & 3] = MFMA16(wf[i & 3][i >> 4], xf[(i >> 2) & 3][i >> 4], acc[i & 3][(i >> 2) & 3]);
                if (i == 19) { xb += adv; PIN(xb); }
                if (i >= 20 && i < 24) { wb[i & 3] += adv; PIN(wb[i & 3]); }
                SBAR();
            }
            if (KIND == 0) BAR();
            SBAR();
            adv = -adv;
        };
        if (!grp) { body(IC<1>{}); BAR(); }
#pragma unroll 1
        for (int seg = 0, n = 2; seg < 3; ++seg, n = 7) {
#pragma unroll 1
            for (int t = 0; t < n; ++t) {
                body(IC<0>{});
            }
            asm volatile("s_waitcnt vmcnt(8)" : "+v"(f3)::"memory");
            if (any(diff(f3, tag))) wait_flags(R, f3o, tag);
            if (seg == 0 && lane < 28) f3o += 28 * 8;
            bw = w44 + seg * 131072;
            bstep = 4 * 131072;
        }
#pragma unroll 1
        for (int t = 0; t <= grp; ++t) {
            body(IC<1>{});
            if (t == 0) BAR();
        }
        u32x4 xr[2];
        xr[0] = ldw4(R, lane * 16, WS_X1 + (mblk * 32 + nt4 * 4 + wn * 2) * 1024);
        xr[1] = ldw4(R, lane * 16 + 1024, WS_X1 + (mblk * 32 + nt4 * 4 + wn * 2) * 1024);
        og = ldw2(R, go, 0);
        f32x4 res[4];
        {
            LDS_AS f32x4* ex = (LDS_AS f32x4*)((lds_ptr)smem + wid * 8192) + lane;
            const LDS_AS f32x4* ey = (const LDS_AS f32x4*)((lds_ptr)smem + (wid ^ 4) * 8192) + lane;
#pragma unroll
            for (int i = 0; i < 4; ++i) ex[i * 64] = acc[i][3];
            LG_BAR();
#pragma unroll
            for (int i = 0; i < 4; ++i) res[i] = ey[i * 64];
#pragma unroll
            for (int i = 0; i < 4; ++i) ex[256 + i * 64] = acc[i][2];
#pragma unroll
            for (int p = 0; p < 2; ++p)
                stw4(R, lane * 16 + p * 1024, WS_EX + b * 16384 + wid * 2048,
                     __builtin_elementwise_min(pack44(res[2 * p] + acc[2 * p][1], res[2 * p + 1] + acc[2 * p + 1][1]), (u32x4){~1u, ~1u, ~1u, ~1u}));
            LG_BAR();
#pragma unroll
            for (int i = 0; i < 4; ++i) res[i] = ey[256 + i * 64] + acc[i][0];
        }
        u32x4 pv[2];
#pragma unroll 1
        for (unsigned n = 0; n < SPIN_CAP; ++n) {
            asm volatile("" ::: "memory");
#pragma unroll
            for (int p = 0; p < 2; ++p) pv[p] = ldw4(R, lane * 16 + p * 1024, WS_EX + pb * 16384 + wid * 2048);
            const u32x4 mx = __builtin_elementwise_max(pv[0], pv[1]);
            if (!any(max(max(mx[0], mx[1]), max(mx[2], mx[3])) == ~0u)) break;
        }
        RETRY(og[1] ^ tag[0], og = ldw2(R, go, 0));
        const float omg = bfhi(og[0]);
#pragma unroll
        for (int p = 0; p < 2; ++p) {
            const f32x4 lo = {bflo(xr[p][0]), bfhi(xr[p][0]), bflo(xr[p][1]), bfhi(xr[p][1])};
            const f32x4 hi = {bflo(xr[p][2]), bfhi(xr[p][2]), bflo(xr[p][3]), bfhi(xr[p][3])};
            const u32x4 o = pack44(lo * omg + (res[2 * p] + (f32x4){bflo(pv[p][0]), bfhi(pv[p][0]), bflo(pv[p][1]), bfhi(pv[p][1])}),
                                   hi * omg + (res[2 * p + 1] + (f32x4){bflo(pv[p][2]), bfhi(pv[p][2]), bflo(pv[p][3]), bfhi(pv[p][3])}));
            auto u = __builtin_amdgcn_permlane16_swap(o[0], o[2], false, false);
            auto v = __builtin_amdgcn_permlane16_swap(o[1], o[3], false, false);
            *(u32x4*)(out + (size_t)(mblk * 16 + c) * D_ + nt4 * 128 + wn * 64 + p * 32 + (g & 1) * 16 + (g >> 1) * 8) =
                (u32x4){u[0], v[0], u[1], v[1]};
        }
        if (b == 0 && wid == 0) stw2(R, lane ? NOWHERE : 0, WS_EPOCH, tag);
    }
}

extern "C" void kernel_launch(void* const* d_in, const int* in_sizes, int n_in, void* d_out, int out_size, void* d_ws,
                              size_t ws_size, hipStream_t stream) {
    (void)hipFuncSetAttribute((const void*)k_mega, hipFuncAttributeMaxDynamicSharedMemorySize, LDS_BYTES);
    k_mega<<<256, 512, LDS_BYTES, stream>>>((const u16*)d_in[0], (const u16*)d_in[1], (const u16*)d_in[2], (const u16*)d_in[4],
                                             (const u16*)d_in[5], (const u16*)d_in[6], (const u16*)d_in[7], (const u16*)d_in[8],
                                             (char*)d_ws, (u16*)d_out);
}
```
